```python
import torch

def _softmax(x, axis=-1):
    return torch.softmax(x, dim=axis)
def _gelu(x):
    return torch.nn.functional.gelu(x, approximate="tanh")
def _silu(x):
    return torch.nn.functional.silu(x)
def _sigmoid(x):
    return torch.sigmoid(x)
def _layernorm(x, axis=-1, eps=1e-5):
    m = x.mean(dim=axis, keepdim=True)
    v = x.var(dim=axis, keepdim=True, unbiased=False)
    return (x - m) / torch.sqrt(v + eps)
def _scatter_add(tgt, idx, src, axis=0):
    return tgt.clone().index_add_(axis, idx, src)
def _segment_sum(src, seg, n):
    out = torch.zeros((n,) + src.shape[1:], dtype=src.dtype)
    return out.index_add_(0, seg, src)
def _pad(x, pw):
    flat = tuple(v for pair in reversed(pw) for v in pair)
    return torch.nn.functional.pad(x, flat)

kernel_name = 'gated_mix__mha_block__sliding_block__prompt'

def setup_inputs():

    _w = (lambda a: (lambda t: t.to(torch.bfloat16) if t.is_floating_point() else t)(torch.as_tensor(a)))
    def _A_setup():
        B, S, D, H = 1, 2048, 512, 32
        return {
            "x": _w((lambda *s: torch.randn(*s, dtype=torch.bfloat16) * 0.05)(B, S, D)),
            "wqkv": _w((lambda *s: torch.randn(*s, dtype=torch.bfloat16) * 1.6)(D, 3 * D)),
            "wo": _w((lambda *s: torch.randn(*s, dtype=torch.bfloat16) * 0.1)(D, D)),
        }
    _A_in = _A_setup()

    def _B_setup():
        B, S, D = 1, 2048, 512
        return {
            "x": _w((lambda *s: torch.randn(*s, dtype=torch.bfloat16) * 0.05)(B, S, D)),
            "wqkv": _w((lambda *s: torch.randn(*s, dtype=torch.bfloat16) * 1.6)(D, 3 * D)),
            "wo": _w((lambda *s: torch.randn(*s, dtype=torch.bfloat16) * 0.05)(D, D)),
        }
    _B_in = _B_setup()

    return {
        "A_x": _A_in["x"],
        "A_wqkv": _A_in["wqkv"],
        "A_wo": _A_in["wo"],
        "B_x": _B_in["x"],
        "B_wqkv": _B_in["wqkv"],
        "B_wo": _B_in["wo"],
        "gate_w": _w((lambda *s: torch.randn(*s, dtype=torch.bfloat16) * 0.05)(512) * 16.0),
    }

def reference(A_x, A_wqkv, A_wo, B_x, B_wqkv, B_wo, gate_w):
    def _A_ref(x, wqkv, wo):
        B, S, D = x.shape
        H = 32
        Dh = D // H
        qkv = (lambda x, *d: x.permute(*d))((x @ wqkv).reshape(B, S, 3, H, Dh), 2, 0, 3, 1, 4)
        q, k, v = qkv[0], qkv[1], qkv[2]
        scores = torch.einsum("bhqd,bhkd->bhqk", q, k) * (Dh ** -0.5)
        p = _softmax(scores, axis=-1)
        o = torch.einsum("bhqk,bhkd->bhqd", p, v)
        return (lambda x, *d: x.permute(*d))(o, 0, 2, 1, 3).reshape(B, S, D) @ wo

    def _B_ref(x, wqkv, wo):
        B, S, D = x.shape
        H = 32; Dh = D // H; W = 256
        qkv = (lambda x, *d: x.permute(*d))((x @ wqkv).reshape(B, S, 3, H, Dh), 2, 0, 3, 1, 4)
        q, k, v = qkv[0], qkv[1], qkv[2]
        scores = torch.einsum("bhqd,bhkd->bhqk", q, k) * (Dh ** -0.5)
        idx = torch.arange(S)
        mask = (idx[:, None] - idx[None, :] >= 0) & (idx[:, None] - idx[None, :] < W)
        scores = torch.where(mask[None, None], scores, -1e9)
        p = _softmax(scores, axis=-1)
        o = torch.einsum("bhqk,bhkd->bhqd", p, v)
        return (lambda x, *d: x.permute(*d))(o, 0, 2, 1, 3).reshape(B, S, D) @ wo

    x = A_x
    g = _sigmoid((x * gate_w).sum(-1, keepdims=True))
    a = _A_ref(x, A_wqkv, A_wo)
    b = _B_ref(x, B_wqkv, B_wo)
    return g * a + (1 - g) * b
```

```cpp
#include <hip/hip_runtime.h>
#include <stdint.h>

typedef __attribute__((ext_vector_type(8))) __bf16 bf16x8;
typedef __attribute__((ext_vector_type(16))) __bf16 bf16x16;
typedef __attribute__((ext_vector_type(2))) __bf16 bf16x2;
typedef __attribute__((ext_vector_type(4))) short s16x4;
typedef __attribute__((ext_vector_type(2))) float f32x2;
typedef __attribute__((ext_vector_type(4))) float f32x4;
typedef __attribute__((ext_vector_type(8))) float f32x8;
typedef __attribute__((ext_vector_type(16))) float f32x16;
typedef __attribute__((ext_vector_type(2))) unsigned u32x2;
typedef __attribute__((ext_vector_type(4))) unsigned u32x4;
typedef __attribute__((ext_vector_type(32))) unsigned u32x32;
typedef unsigned long long u64;
#define LAS __attribute__((address_space(3)))
#define GAS __attribute__((address_space(1)))
#define DI __device__ __forceinline__
#define RLX_AGENT __ATOMIC_RELAXED, __HIP_MEMORY_SCOPE_AGENT

#define ATTN_A_ASM \
    "s_mov_b32 s40, %[b0]\n\t" \
    "s_mov_b32 s41, %[b1]\n\t" \
    "s_mov_b32 s42, %[nrec]\n\t" \
    "s_mov_b32 s43, 0x20000\n\t" \
    "s_mov_b32 s44, 0xffff\n\t" \
    "s_mov_b32 s45, 0xffff\n\t" \
    "s_mov_b32 s46, %[so]\n\t" \
    "buffer_load_dwordx4 v[176:179], %[vk], s[40:43], s46 offen offset:0 sc1\n\t" \
    "s_mov_b64 exec, s[44:45]\n\t" \
    "buffer_load_dwordx4 v[192:195], %[vv], s[40:43], s46 offen offset:1024 sc1\n\t" \
    "buffer_load_dwordx4 v[196:199], %[vv], s[40:43], s46 offen offset:1536 sc1\n\t" \
    "s_mov_b64 exec, -1\n\t" \
    "buffer_load_dwordx4 v[180:183], %[vk], s[40:43], s46 offen offset:2048 sc1\n\t" \
    "s_mov_b64 exec, s[44:45]\n\t" \
    "buffer_load_dwordx4 v[200:203], %[vv], s[40:43], s46 offen offset:3072 sc1\n\t" \
    "buffer_load_dwordx4 v[204:207], %[vv], s[40:43], s46 offen offset:3584 sc1\n\t" \
    "s_mov_b64 exec, -1\n\t" \
    "s_add_u32 s46, s46, 0x1000\n\t" \
    "s_and_b32 s46, s46, 0xffff\n\t" \
    "buffer_load_dwordx4 v[184:187], %[vk], s[40:43], s46 offen offset:0 sc1\n\t" \
    "s_mov_b64 exec, s[44:45]\n\t" \
    "buffer_load_dwordx4 v[208:211], %[vv], s[40:43], s46 offen offset:1024 sc1\n\t" \
    "buffer_load_dwordx4 v[212:215], %[vv], s[40:43], s46 offen offset:1536 sc1\n\t" \
    "s_mov_b64 exec, -1\n\t" \
    "s_mov_b32 s47, 4\n\t" \
    "s_mov_b64 vcc, 0\n\t" \
    "s_waitcnt vmcnt(6)\n\t" \
    "v_mfma_f32_32x32x16_bf16 v[224:239], v[176:179], %[q0], 0\n\t" \
    "s_nop 11\n\t" \
    "v_exp_f32 v224, v224\n\t" \
    "v_exp_f32 v225, v225\n\t" \
    "s_branch 2f\n\t" \
    ".p2align 6\n\t" \
    "1:\n\t" \
    "v_exp_f32 v224, v224\n\t" \
    "v_exp_f32 v225, v225\n\t" \
    "v_smfmac_f32_32x32x32_bf16 %[o1], v[220:223], v[248:255], %[idx]\n\t" \
    "2:\n\t" \
    "s_mov_b64 exec, s[44:45]\n\t" \
    "buffer_load_dwordx4 v[216:219], %[vv], s[40:43], s46 offen offset:3072 sc1\n\t" \
    "buffer_load_dwordx4 v[220:223], %[vv], s[40:43], s46 offen offset:3584 sc1\n\t" \
    "s_mov_b64 exec, -1\n\t" \
    "v_exp_f32 v226, v226\n\t" \
    "v_exp_f32 v227, v227\n\t" \
    "v_mfma_f32_32x32x16_bf16 v[240:255], v[176:179], %[q1], 0\n\t" \
    "buffer_load_dwordx4 v[188:191], %[vk], s[40:43], s46 offen offset:2048 sc1\n\t" \
    "v_exp_f32 v228, v228\n\t" \
    "v_exp_f32 v229, v229\n\t" \
    "v_exp_f32 v230, v230\n\t" \
    "v_exp_f32 v231, v231\n\t" \
    "v_exp_f32 v232, v232\n\t" \
    "v_exp_f32 v233, v233\n\t" \
    "v_smfmac_f32_32x32x32_bf16 %[o0], v[192:195], v[224:231], %[idx]\n\t" \
    "s_cmp_eq_u32 s47, 1\n\t" \
    "s_cbranch_scc1 3f\n\t" \
    "4:\n\t" \
    "v_exp_f32 v234, v234\n\t" \
    "v_exp_f32 v235, v235\n\t" \
    "v_exp_f32 v236, v236\n\t" \
    "v_exp_f32 v237, v237\n\t" \
    "v_exp_f32 v238, v238\n\t" \
    "v_exp_f32 v239, v239\n\t" \
    "v_exp_f32 v240, v240\n\t" \
    "v_exp_f32 v241, v241\n\t" \
    "v_smfmac_f32_32x32x32_bf16 %[o0], v[196:199], v[232:239], %[idx]\n\t" \
    "s_add_u32 s46, s46, 0x1000\n\t" \
    "s_and_b32 s46, s46, 0xffff\n\t" \
    "v_exp_f32 v242, v242\n\t" \
    "v_exp_f32 v243, v243\n\t" \
    "s_waitcnt vmcnt(6)\n\t" \
    "v_mfma_f32_32x32x16_bf16 v[224:239], v[180:183], %[q0], 0\n\t" \
    "v_exp_f32 v244, v244\n\t" \
    "v_exp_f32 v245, v245\n\t" \
    "v_exp_f32 v246, v246\n\t" \
    "v_exp_f32 v247, v247\n\t" \
    "v_exp_f32 v248, v248\n\t" \
    "v_exp_f32 v249, v249\n\t" \
    "v_smfmac_f32_32x32x32_bf16 %[o1], v[192:195], v[240:247], %[idx]\n\t" \
    "v_exp_f32 v250, v250\n\t" \
    "v_exp_f32 v251, v251\n\t" \
    "v_exp_f32 v252, v252\n\t" \
    "v_exp_f32 v253, v253\n\t" \
    "v_exp_f32 v254, v254\n\t" \
    "v_exp_f32 v255, v255\n\t" \
    "v_exp_f32 v224, v224\n\t" \
    "v_exp_f32 v225, v225\n\t" \
    "v_smfmac_f32_32x32x32_bf16 %[o1], v[196:199], v[248:255], %[idx]\n\t" \
    "s_mov_b64 exec, s[44:45]\n\t" \
    "buffer_load_dwordx4 v[192:195], %[vv], s[40:43], s46 offen offset:1024 sc1\n\t" \
    "buffer_load_dwordx4 v[196:199], %[vv], s[40:43], s46 offen offset:1536 sc1\n\t" \
    "s_mov_b64 exec, -1\n\t" \
    "v_exp_f32 v226, v226\n\t" \
    "v_exp_f32 v227, v227\n\t" \
    "v_mfma_f32_32x32x16_bf16 v[240:255], v[180:183], %[q1], 0\n\t" \
    "buffer_load_dwordx4 v[176:179], %[vk], s[40:43], s46 offen offset:0 sc1\n\t" \
    "v_exp_f32 v228, v228\n\t" \
    "v_exp_f32 v229, v229\n\t" \
    "v_exp_f32 v230, v230\n\t" \
    "v_exp_f32 v231, v231\n\t" \
    "v_exp_f32 v232, v232\n\t" \
    "v_exp_f32 v233, v233\n\t" \
    "v_smfmac_f32_32x32x32_bf16 %[o0], v[200:203], v[224:231], %[idx]\n\t" \
    "v_exp_f32 v234, v234\n\t" \
    "v_exp_f32 v235, v235\n\t" \
    "v_exp_f32 v236, v236\n\t" \
    "v_exp_f32 v237, v237\n\t" \
    "v_exp_f32 v238, v238\n\t" \
    "v_exp_f32 v239, v239\n\t" \
    "v_exp_f32 v240, v240\n\t" \
    "v_exp_f32 v241, v241\n\t" \
    "v_smfmac_f32_32x32x32_bf16 %[o0], v[204:207], v[232:239], %[idx]\n\t" \
    "s_cbranch_vccnz 5f\n\t" \
    "6:\n\t" \
    "v_exp_f32 v242, v242\n\t" \
    "v_exp_f32 v243, v243\n\t" \
    "s_waitcnt vmcnt(6)\n\t" \
    "v_mfma_f32_32x32x16_bf16 v[224:239], v[184:187], %[q0], 0\n\t" \
    "v_exp_f32 v244, v244\n\t" \
    "v_exp_f32 v245, v245\n\t" \
    "v_exp_f32 v246, v246\n\t" \
    "v_exp_f32 v247, v247\n\t" \
    "v_exp_f32 v248, v248\n\t" \
    "v_exp_f32 v249, v249\n\t" \
    "v_smfmac_f32_32x32x32_bf16 %[o1], v[200:203], v[240:247], %[idx]\n\t" \
    "v_exp_f32 v250, v250\n\t" \
    "v_exp_f32 v251, v251\n\t" \
    "v_exp_f32 v252, v252\n\t" \
    "v_exp_f32 v253, v253\n\t" \
    "v_exp_f32 v254, v254\n\t" \
    "v_exp_f32 v255, v255\n\t" \
    "v_exp_f32 v224, v224\n\t" \
    "v_exp_f32 v225, v225\n\t" \
    "v_smfmac_f32_32x32x32_bf16 %[o1], v[204:207], v[248:255], %[idx]\n\t" \
    "s_mov_b64 exec, s[44:45]\n\t" \
    "buffer_load_dwordx4 v[200:203], %[vv], s[40:43], s46 offen offset:3072 sc1\n\t" \
    "buffer_load_dwordx4 v[204:207], %[vv], s[40:43], s46 offen offset:3584 sc1\n\t" \
    "s_mov_b64 exec, -1\n\t" \
    "v_exp_f32 v226, v226\n\t" \
    "v_exp_f32 v227, v227\n\t" \
    "v_mfma_f32_32x32x16_bf16 v[240:255], v[184:187], %[q1], 0\n\t" \
    "buffer_load_dwordx4 v[180:183], %[vk], s[40:43], s46 offen offset:2048 sc1\n\t" \
    "v_exp_f32 v228, v228\n\t" \
    "v_exp_f32 v229, v229\n\t" \
    "v_exp_f32 v230, v230\n\t" \
    "v_exp_f32 v231, v231\n\t" \
    "v_exp_f32 v232, v232\n\t" \
    "v_exp_f32 v233, v233\n\t" \
    "v_smfmac_f32_32x32x32_bf16 %[o0], v[208:211], v[224:231], %[idx]\n\t" \
    "v_exp_f32 v234, v234\n\t" \
    "v_exp_f32 v235, v235\n\t" \
    "v_exp_f32 v236, v236\n\t" \
    "v_exp_f32 v237, v237\n\t" \
    "v_exp_f32 v238, v238\n\t" \
    "v_exp_f32 v239, v239\n\t" \
    "v_exp_f32 v240, v240\n\t" \
    "v_exp_f32 v241, v241\n\t" \
    "v_smfmac_f32_32x32x32_bf16 %[o0], v[212:215], v[232:239], %[idx]\n\t" \
    "s_add_u32 s46, s46, 0x1000\n\t" \
    "s_and_b32 s46, s46, 0xffff\n\t" \
    "v_exp_f32 v242, v242\n\t" \
    "v_exp_f32 v243, v243\n\t" \
    "s_waitcnt vmcnt(6)\n\t" \
    "v_mfma_f32_32x32x16_bf16 v[224:239], v[188:191], %[q0], 0\n\t" \
    "v_exp_f32 v244, v244\n\t" \
    "v_exp_f32 v245, v245\n\t" \
    "v_exp_f32 v246, v246\n\t" \
    "v_exp_f32 v247, v247\n\t" \
    "v_exp_f32 v248, v248\n\t" \
    "v_exp_f32 v249, v249\n\t" \
    "v_smfmac_f32_32x32x32_bf16 %[o1], v[208:211], v[240:247], %[idx]\n\t" \
    "v_exp_f32 v250, v250\n\t" \
    "v_exp_f32 v251, v251\n\t" \
    "v_exp_f32 v252, v252\n\t" \
    "v_exp_f32 v253, v253\n\t" \
    "v_exp_f32 v254, v254\n\t" \
    "v_exp_f32 v255, v255\n\t" \
    "v_exp_f32 v224, v224\n\t" \
    "v_exp_f32 v225, v225\n\t" \
    "v_smfmac_f32_32x32x32_bf16 %[o1], v[212:215], v[248:255], %[idx]\n\t" \
    "s_mov_b64 exec, s[44:45]\n\t" \
    "buffer_load_dwordx4 v[208:211], %[vv], s[40:43], s46 offen offset:1024 sc1\n\t" \
    "buffer_load_dwordx4 v[212:215], %[vv], s[40:43], s46 offen offset:1536 sc1\n\t" \
    "s_mov_b64 exec, -1\n\t" \
    "v_exp_f32 v226, v226\n\t" \
    "v_exp_f32 v227, v227\n\t" \
    "v_mfma_f32_32x32x16_bf16 v[240:255], v[188:191], %[q1], 0\n\t" \
    "buffer_load_dwordx4 v[184:187], %[vk], s[40:43], s46 offen offset:0 sc1\n\t" \
    "v_exp_f32 v228, v228\n\t" \
    "v_exp_f32 v229, v229\n\t" \
    "v_exp_f32 v230, v230\n\t" \
    "v_exp_f32 v231, v231\n\t" \
    "v_exp_f32 v232, v232\n\t" \
    "v_exp_f32 v233, v233\n\t" \
    "v_smfmac_f32_32x32x32_bf16 %[o0], v[216:219], v[224:231], %[idx]\n\t" \
    "v_exp_f32 v234, v234\n\t" \
    "v_exp_f32 v235, v235\n\t" \
    "v_exp_f32 v236, v236\n\t" \
    "v_exp_f32 v237, v237\n\t" \
    "v_exp_f32 v238, v238\n\t" \
    "v_exp_f32 v239, v239\n\t" \
    "v_exp_f32 v240, v240\n\t" \
    "v_exp_f32 v241, v241\n\t" \
    "v_smfmac_f32_32x32x32_bf16 %[o0], v[220:223], v[232:239], %[idx]\n\t" \
    "s_add_i32 s47, s47, -1\n\t" \
    "v_exp_f32 v242, v242\n\t" \
    "v_exp_f32 v243, v243\n\t" \
    "s_waitcnt vmcnt(6)\n\t" \
    "v_mfma_f32_32x32x16_bf16 v[224:239], v[176:179], %[q0], 0\n\t" \
    "v_exp_f32 v244, v244\n\t" \
    "v_exp_f32 v245, v245\n\t" \
    "v_exp_f32 v246, v246\n\t" \
    "v_exp_f32 v247, v247\n\t" \
    "v_exp_f32 v248, v248\n\t" \
    "v_exp_f32 v249, v249\n\t" \
    "v_smfmac_f32_32x32x32_bf16 %[o1], v[216:219], v[240:247], %[idx]\n\t" \
    "s_cmp_eq_u32 s47, 0\n\t" \
    "v_exp_f32 v250, v250\n\t" \
    "v_exp_f32 v251, v251\n\t" \
    "v_exp_f32 v252, v252\n\t" \
    "v_exp_f32 v253, v253\n\t" \
    "v_exp_f32 v254, v254\n\t" \
    "v_exp_f32 v255, v255\n\t" \
    "s_cbranch_scc0 1b\n\t" \
    "s_nop 1\n\t" \
    "v_smfmac_f32_32x32x32_bf16 %[o1], v[220:223], v[248:255], %[idx]\n\t" \
    "s_branch 7f\n\t" \
    "3:\n\t" \
    "s_mov_b32 s42, 0\n\t" \
    "s_cmp_eq_u32 %[fin], 0\n\t" \
    "s_cbranch_scc1 4b\n\t" \
    "s_setprio 1\n\t" \
    "s_mov_b64 vcc, -1\n\t" \
    "s_branch 4b\n\t" \
    "5:\n\t" \
    "s_setprio 0\n\t" \
    "s_branch 6b\n\t" \
    "7:\n\t" \
    "s_waitcnt vmcnt(0)\n\t" \
    "s_nop 11\n\t" \
    ""
#define ATTN_B_ASM \
    "s_mov_b32 s40, %[b0]\n\t" \
    "s_mov_b32 s41, %[b1]\n\t" \
    "s_mov_b32 s42, %[nrec]\n\t" \
    "s_mov_b32 s43, 0x20000\n\t" \
    "s_mov_b32 s44, 0xffff\n\t" \
    "s_mov_b32 s45, 0xffff\n\t" \
    "s_mov_b32 s46, %[so]\n\t" \
    "buffer_load_dwordx4 v[176:179], %[vk], s[40:43], s46 offen offset:0 sc1\n\t" \
    "s_mov_b64 exec, s[44:45]\n\t" \
    "buffer_load_dwordx4 v[192:195], %[vv], s[40:43], s46 offen offset:1024 sc1\n\t" \
    "buffer_load_dwordx4 v[196:199], %[vv], s[40:43], s46 offen offset:1536 sc1\n\t" \
    "s_mov_b64 exec, -1\n\t" \
    "s_sub_i32 s46, s46, 0x800\n\t" \
    "s_max_i32 s46, s46, 0\n\t" \
    "buffer_load_dwordx4 v[180:183], %[vk], s[40:43], s46 offen offset:0 sc1\n\t" \
    "s_mov_b64 exec, s[44:45]\n\t" \
    "buffer_load_dwordx4 v[200:203], %[vv], s[40:43], s46 offen offset:1024 sc1\n\t" \
    "buffer_load_dwordx4 v[204:207], %[vv], s[40:43], s46 offen offset:1536 sc1\n\t" \
    "s_mov_b64 exec, -1\n\t" \
    "s_sub_i32 s46, s46, 0x800\n\t" \
    "s_max_i32 s46, s46, 0\n\t" \
    "buffer_load_dwordx4 v[184:187], %[vk], s[40:43], s46 offen offset:0 sc1\n\t" \
    "s_mov_b64 exec, s[44:45]\n\t" \
    "buffer_load_dwordx4 v[208:211], %[vv], s[40:43], s46 offen offset:1024 sc1\n\t" \
    "buffer_load_dwordx4 v[212:215], %[vv], s[40:43], s46 offen offset:1536 sc1\n\t" \
    "s_mov_b64 exec, -1\n\t" \
    "s_sub_i32 s46, s46, 0x800\n\t" \
    "s_max_i32 s46, s46, 0\n\t" \
    "buffer_load_dwordx4 v[188:191], %[vk], s[40:43], s46 offen offset:0 sc1\n\t" \
    "s_mov_b64 exec, s[44:45]\n\t" \
    "buffer_load_dwordx4 v[216:219], %[vv], s[40:43], s46 offen offset:1024 sc1\n\t" \
    "buffer_load_dwordx4 v[220:223], %[vv], s[40:43], s46 offen offset:1536 sc1\n\t" \
    "s_mov_b64 exec, -1\n\t" \
    "s_sub_i32 s46, s46, 0x800\n\t" \
    "s_max_i32 s46, s46, 0\n\t" \
    "s_waitcnt vmcnt(9)\n\t" \
    "v_mfma_f32_32x32x16_bf16 v[224:239], v[176:179], %[q0], %[md]\n\t" \
    "s_nop 11\n\t" \
    "v_exp_f32 v224, v224\n\t" \
    "v_exp_f32 v225, v225\n\t" \
    "v_exp_f32 v226, v226\n\t" \
    "v_exp_f32 v227, v227\n\t" \
    "s_waitcnt vmcnt(6)\n\t" \
    "v_mfma_f32_32x32x16_bf16 v[240:255], v[180:183], %[q0], 0\n\t" \
    "v_exp_f32 v228, v228\n\t" \
    "v_exp_f32 v229, v229\n\t" \
    "v_exp_f32 v230, v230\n\t" \
    "v_exp_f32 v231, v231\n\t" \
    "v_exp_f32 v232, v232\n\t" \
    "v_exp_f32 v233, v233\n\t" \
    "v_smfmac_f32_32x32x32_bf16 %[o0], v[192:195], v[224:231], %[idx]\n\t" \
    "v_exp_f32 v234, v234\n\t" \
    "v_exp_f32 v235, v235\n\t" \
    "v_exp_f32 v236, v236\n\t" \
    "v_exp_f32 v237, v237\n\t" \
    "v_exp_f32 v238, v238\n\t" \
    "v_exp_f32 v239, v239\n\t" \
    "v_exp_f32 v240, v240\n\t" \
    "v_exp_f32 v241, v241\n\t" \
    "v_smfmac_f32_32x32x32_bf16 %[o0], v[196:199], v[232:239], %[idx]\n\t" \
    "s_cmp_eq_u32 %[nt], 1\n\t" \
    "s_cbranch_scc1 7f\n\t" \
    "s_mov_b64 exec, s[44:45]\n\t" \
    "buffer_load_dwordx4 v[192:195], %[vv], s[40:43], s46 offen offset:1024 sc1\n\t" \
    "buffer_load_dwordx4 v[196:199], %[vv], s[40:43], s46 offen offset:1536 sc1\n\t" \
    "s_mov_b64 exec, -1\n\t" \
    "buffer_load_dwordx4 v[176:179], %[vk], s[40:43], s46 offen offset:0 sc1\n\t" \
    "s_sub_i32 s46, s46, 0x800\n\t" \
    "s_max_i32 s46, s46, 0\n\t" \
    "v_exp_f32 v242, v242\n\t" \
    "v_exp_f32 v243, v243\n\t" \
    "s_waitcnt vmcnt(6)\n\t" \
    "v_mfma_f32_32x32x16_bf16 v[224:239], v[184:187], %[q0], 0\n\t" \
    "v_exp_f32 v244, v244\n\t" \
    "v_exp_f32 v245, v245\n\t" \
    "v_exp_f32 v246, v246\n\t" \
    "v_exp_f32 v247, v247\n\t" \
    "v_exp_f32 v248, v248\n\t" \
    "v_exp_f32 v249, v249\n\t" \
    "v_smfmac_f32_32x32x32_bf16 %[o0], v[200:203], v[240:247], %[idx]\n\t" \
    "v_exp_f32 v250, v250\n\t" \
    "v_exp_f32 v251, v251\n\t" \
    "v_exp_f32 v252, v252\n\t" \
    "v_exp_f32 v253, v253\n\t" \
    "v_exp_f32 v254, v254\n\t" \
    "v_exp_f32 v255, v255\n\t" \
    "v_exp_f32 v224, v224\n\t" \
    "v_exp_f32 v225, v225\n\t" \
    "v_smfmac_f32_32x32x32_bf16 %[o0], v[204:207], v[248:255], %[idx]\n\t" \
    "s_cmp_eq_u32 %[nt], 2\n\t" \
    "s_cbranch_scc1 7f\n\t" \
    "s_mov_b64 exec, s[44:45]\n\t" \
    "buffer_load_dwordx4 v[200:203], %[vv], s[40:43], s46 offen offset:1024 sc1\n\t" \
    "buffer_load_dwordx4 v[204:207], %[vv], s[40:43], s46 offen offset:1536 sc1\n\t" \
    "s_mov_b64 exec, -1\n\t" \
    "buffer_load_dwordx4 v[180:183], %[vk], s[40:43], s46 offen offset:0 sc1\n\t" \
    "s_sub_i32 s46, s46, 0x800\n\t" \
    "s_max_i32 s46, s46, 0\n\t" \
    "v_exp_f32 v226, v226\n\t" \
    "v_exp_f32 v227, v227\n\t" \
    "s_waitcnt vmcnt(6)\n\t" \
    "v_mfma_f32_32x32x16_bf16 v[240:255], v[188:191], %[q0], 0\n\t" \
    "v_exp_f32 v228, v228\n\t" \
    "v_exp_f32 v229, v229\n\t" \
    "v_exp_f32 v230, v230\n\t" \
    "v_exp_f32 v231, v231\n\t" \
    "v_exp_f32 v232, v232\n\t" \
    "v_exp_f32 v233, v233\n\t" \
    "v_smfmac_f32_32x32x32_bf16 %[o0], v[208:211], v[224:231], %[idx]\n\t" \
    "v_exp_f32 v234, v234\n\t" \
    "v_exp_f32 v235, v235\n\t" \
    "v_exp_f32 v236, v236\n\t" \
    "v_exp_f32 v237, v237\n\t" \
    "v_exp_f32 v238, v238\n\t" \
    "v_exp_f32 v239, v239\n\t" \
    "v_exp_f32 v240, v240\n\t" \
    "v_exp_f32 v241, v241\n\t" \
    "v_smfmac_f32_32x32x32_bf16 %[o0], v[212:215], v[232:239], %[idx]\n\t" \
    "s_cmp_eq_u32 %[nt], 3\n\t" \
    "s_cbranch_scc1 7f\n\t" \
    "s_mov_b64 exec, s[44:45]\n\t" \
    "buffer_load_dwordx4 v[208:211], %[vv], s[40:43], s46 offen offset:1024 sc1\n\t" \
    "buffer_load_dwordx4 v[212:215], %[vv], s[40:43], s46 offen offset:1536 sc1\n\t" \
    "s_mov_b64 exec, -1\n\t" \
    "buffer_load_dwordx4 v[184:187], %[vk], s[40:43], s46 offen offset:0 sc1\n\t" \
    "s_sub_i32 s46, s46, 0x800\n\t" \
    "s_max_i32 s46, s46, 0\n\t" \
    "v_exp_f32 v242, v242\n\t" \
    "v_exp_f32 v243, v243\n\t" \
    "s_waitcnt vmcnt(6)\n\t" \
    "v_mfma_f32_32x32x16_bf16 v[224:239], v[176:179], %[q0], 0\n\t" \
    "v_exp_f32 v244, v244\n\t" \
    "v_exp_f32 v245, v245\n\t" \
    "v_exp_f32 v246, v246\n\t" \
    "v_exp_f32 v247, v247\n\t" \
    "v_exp_f32 v248, v248\n\t" \
    "v_exp_f32 v249, v249\n\t" \
    "v_smfmac_f32_32x32x32_bf16 %[o0], v[216:219], v[240:247], %[idx]\n\t" \
    "v_exp_f32 v250, v250\n\t" \
    "v_exp_f32 v251, v251\n\t" \
    "v_exp_f32 v252, v252\n\t" \
    "v_exp_f32 v253, v253\n\t" \
    "v_exp_f32 v254, v254\n\t" \
    "v_exp_f32 v255, v255\n\t" \
    "v_exp_f32 v224, v224\n\t" \
    "v_exp_f32 v225, v225\n\t" \
    "v_smfmac_f32_32x32x32_bf16 %[o0], v[220:223], v[248:255], %[idx]\n\t" \
    "s_cmp_eq_u32 %[nt], 4\n\t" \
    "s_cbranch_scc1 7f\n\t" \
    "s_mov_b64 exec, s[44:45]\n\t" \
    "buffer_load_dwordx4 v[216:219], %[vv], s[40:43], s46 offen offset:1024 sc1\n\t" \
    "buffer_load_dwordx4 v[220:223], %[vv], s[40:43], s46 offen offset:1536 sc1\n\t" \
    "s_mov_b64 exec, -1\n\t" \
    "buffer_load_dwordx4 v[188:191], %[vk], s[40:43], s46 offen offset:0 sc1\n\t" \
    "s_sub_i32 s46, s46, 0x800\n\t" \
    "s_max_i32 s46, s46, 0\n\t" \
    "v_exp_f32 v226, v226\n\t" \
    "v_exp_f32 v227, v227\n\t" \
    "s_waitcnt vmcnt(6)\n\t" \
    "v_mfma_f32_32x32x16_bf16 v[240:255], v[180:183], %[q0], 0\n\t" \
    "v_exp_f32 v228, v228\n\t" \
    "v_exp_f32 v229, v229\n\t" \
    "v_exp_f32 v230, v230\n\t" \
    "v_exp_f32 v231, v231\n\t" \
    "v_exp_f32 v232, v232\n\t" \
    "v_exp_f32 v233, v233\n\t" \
    "v_smfmac_f32_32x32x32_bf16 %[o0], v[192:195], v[224:231], %[idx]\n\t" \
    "v_exp_f32 v234, v234\n\t" \
    "v_exp_f32 v235, v235\n\t" \
    "v_exp_f32 v236, v236\n\t" \
    "v_exp_f32 v237, v237\n\t" \
    "v_exp_f32 v238, v238\n\t" \
    "v_exp_f32 v239, v239\n\t" \
    "v_exp_f32 v240, v240\n\t" \
    "v_exp_f32 v241, v241\n\t" \
    "v_smfmac_f32_32x32x32_bf16 %[o0], v[196:199], v[232:239], %[idx]\n\t" \
    "s_cmp_eq_u32 %[nt], 5\n\t" \
    "s_cbranch_scc1 7f\n\t" \
    "s_mov_b64 exec, s[44:45]\n\t" \
    "buffer_load_dwordx4 v[192:195], %[vv], s[40:43], s46 offen offset:1024 sc1\n\t" \
    "buffer_load_dwordx4 v[196:199], %[vv], s[40:43], s46 offen offset:1536 sc1\n\t" \
    "s_mov_b64 exec, -1\n\t" \
    "buffer_load_dwordx4 v[176:179], %[vk], s[40:43], s46 offen offset:0 sc1\n\t" \
    "s_sub_i32 s46, s46, 0x800\n\t" \
    "s_max_i32 s46, s46, 0\n\t" \
    "v_exp_f32 v242, v242\n\t" \
    "v_exp_f32 v243, v243\n\t" \
    "s_waitcnt vmcnt(6)\n\t" \
    "v_mfma_f32_32x32x16_bf16 v[224:239], v[184:187], %[q0], 0\n\t" \
    "v_exp_f32 v244, v244\n\t" \
    "v_exp_f32 v245, v245\n\t" \
    "v_exp_f32 v246, v246\n\t" \
    "v_exp_f32 v247, v247\n\t" \
    "v_exp_f32 v248, v248\n\t" \
    "v_exp_f32 v249, v249\n\t" \
    "v_smfmac_f32_32x32x32_bf16 %[o0], v[200:203], v[240:247], %[idx]\n\t" \
    "v_exp_f32 v250, v250\n\t" \
    "v_exp_f32 v251, v251\n\t" \
    "v_exp_f32 v252, v252\n\t" \
    "v_exp_f32 v253, v253\n\t" \
    "v_exp_f32 v254, v254\n\t" \
    "v_exp_f32 v255, v255\n\t" \
    "v_exp_f32 v224, v224\n\t" \
    "v_exp_f32 v225, v225\n\t" \
    "v_smfmac_f32_32x32x32_bf16 %[o0], v[204:207], v[248:255], %[idx]\n\t" \
    "s_cmp_eq_u32 %[nt], 6\n\t" \
    "s_cbranch_scc1 7f\n\t" \
    "v_exp_f32 v226, v226\n\t" \
    "v_exp_f32 v227, v227\n\t" \
    "s_waitcnt vmcnt(3)\n\t" \
    "v_mfma_f32_32x32x16_bf16 v[240:255], v[188:191], %[q0], 0\n\t" \
    "v_exp_f32 v228, v228\n\t" \
    "v_exp_f32 v229, v229\n\t" \
    "v_exp_f32 v230, v230\n\t" \
    "v_exp_f32 v231, v231\n\t" \
    "v_exp_f32 v232, v232\n\t" \
    "v_exp_f32 v233, v233\n\t" \
    "v_smfmac_f32_32x32x32_bf16 %[o0], v[208:211], v[224:231], %[idx]\n\t" \
    "v_exp_f32 v234, v234\n\t" \
    "v_exp_f32 v235, v235\n\t" \
    "v_exp_f32 v236, v236\n\t" \
    "v_exp_f32 v237, v237\n\t" \
    "v_exp_f32 v238, v238\n\t" \
    "v_exp_f32 v239, v239\n\t" \
    "v_exp_f32 v240, v240\n\t" \
    "v_exp_f32 v241, v241\n\t" \
    "v_smfmac_f32_32x32x32_bf16 %[o0], v[212:215], v[232:239], %[idx]\n\t" \
    "s_cmp_eq_u32 %[nt], 7\n\t" \
    "s_cbranch_scc1 7f\n\t" \
    "v_exp_f32 v242, v242\n\t" \
    "v_exp_f32 v243, v243\n\t" \
    "s_waitcnt vmcnt(0)\n\t" \
    "v_mfma_f32_32x32x16_bf16 v[224:239], v[176:179], %[q0], %[mf]\n\t" \
    "v_exp_f32 v244, v244\n\t" \
    "v_exp_f32 v245, v245\n\t" \
    "v_exp_f32 v246, v246\n\t" \
    "v_exp_f32 v247, v247\n\t" \
    "v_exp_f32 v248, v248\n\t" \
    "v_exp_f32 v249, v249\n\t" \
    "v_smfmac_f32_32x32x32_bf16 %[o0], v[216:219], v[240:247], %[idx]\n\t" \
    "v_exp_f32 v250, v250\n\t" \
    "v_exp_f32 v251, v251\n\t" \
    "v_exp_f32 v252, v252\n\t" \
    "v_exp_f32 v253, v253\n\t" \
    "v_exp_f32 v254, v254\n\t" \
    "v_exp_f32 v255, v255\n\t" \
    "v_exp_f32 v224, v224\n\t" \
    "v_exp_f32 v225, v225\n\t" \
    "v_smfmac_f32_32x32x32_bf16 %[o0], v[220:223], v[248:255], %[idx]\n\t" \
    "s_cmp_eq_u32 %[nt], 8\n\t" \
    "s_cbranch_scc1 7f\n\t" \
    "v_exp_f32 v226, v226\n\t" \
    "v_exp_f32 v227, v227\n\t" \
    "v_exp_f32 v228, v228\n\t" \
    "v_exp_f32 v229, v229\n\t" \
    "v_exp_f32 v230, v230\n\t" \
    "v_exp_f32 v231, v231\n\t" \
    "v_exp_f32 v232, v232\n\t" \
    "v_exp_f32 v233, v233\n\t" \
    "v_smfmac_f32_32x32x32_bf16 %[o0], v[192:195], v[224:231], %[idx]\n\t" \
    "v_exp_f32 v234, v234\n\t" \
    "v_exp_f32 v235, v235\n\t" \
    "v_exp_f32 v236, v236\n\t" \
    "v_exp_f32 v237, v237\n\t" \
    "v_exp_f32 v238, v238\n\t" \
    "v_exp_f32 v239, v239\n\t" \
    "s_nop 1\n\t" \
    "v_smfmac_f32_32x32x32_bf16 %[o0], v[196:199], v[232:239], %[idx]\n\t" \
    "7:\n\t" \
    "s_waitcnt vmcnt(0)\n\t" \
    "s_nop 11\n\t" \
    ""
#define ATTN_CLOBBERS "v176", "v177", "v178", "v179", "v180", "v181", "v182", "v183", "v184", "v185", "v186", "v187", "v188", "v189", "v190", "v191", "v224", "v225", "v226", "v227", "v228", "v229", "v230", "v231", "v232", "v233", "v234", "v235", "v236", "v237", "v238", "v239", "v240", "v241", "v242", "v243", "v244", "v245", "v246", "v247", "v248", "v249", "v250", "v251", "v252", "v253", "v254", "v255", "s40", "s41", "s42", "s43", "s44", "s45", "s46", "s47", "vcc", "scc", "memory"

constexpr unsigned WS_EPOCH = 0, WS_FKV = 4096  , WS_FO = 12288  ;
constexpr unsigned WS_KV = 1u << 20  , WS_O = 9u << 20  ;
constexpr unsigned WS_PROF = 16u << 20;
constexpr unsigned L_GATEW = 131072, L_GPART = 132096, L_HAND = 133120, L_MTAB = 141312, L_CNT = 141824, L_COMB = 142336, L_TOTAL = 160768;
constexpr unsigned SPIN_CAP = 1u << 20;
constexpr float QSCALE = 0.25f * 1.4426950408889634f;

#define STAMP(i)

DI unsigned pk2(float a, float b) { f32x2 v = {a, b}; return __builtin_bit_cast(unsigned, __builtin_convertvector(v, bf16x2)); }
DI __amdgpu_buffer_rsrc_t mksrd(const void* p, unsigned bytes) { return __builtin_amdgcn_make_buffer_rsrc((void*)p, (short)0, (int)bytes, 0x00020000); }
DI void dma1(unsigned lds, unsigned voff, __amdgpu_buffer_rsrc_t srd, unsigned soff) {
  asm volatile("s_mov_b32 m0, %0\n\ts_nop 0\n\tbuffer_load_dwordx4 %1, %2, %3 offen lds" :: "s"(lds), "v"(voff), "s"(srd), "s"(soff) : "memory");
}
template <int N> DI void wait_vm() { asm volatile("s_waitcnt vmcnt(%0)" :: "i"(N) : "memory"); }
DI void bar() { asm volatile("s_waitcnt lgkmcnt(0)\n\ts_barrier" ::: "memory"); }
DI u32x4 lds_ld16(unsigned a) { return *(const LAS u32x4*)a; }
DI void lds_st16(unsigned a, u32x4 v) { *(LAS u32x4*)a = v; }
DI bf16x8 lds_tr(unsigned a, unsigned second) {
  s16x4 lo = __builtin_amdgcn_ds_read_tr16_b64_v4i16((LAS s16x4*)a), hi = __builtin_amdgcn_ds_read_tr16_b64_v4i16((LAS s16x4*)(a + second));
  return __builtin_bit_cast(bf16x8, __builtin_shufflevector(lo, hi, 0, 1, 2, 3, 4, 5, 6, 7));
}
DI f32x4 mfma16(bf16x8 a, bf16x8 b, f32x4 c) { return __builtin_amdgcn_mfma_f32_16x16x32_bf16(a, b, c, 0, 0, 0); }
DI u32x4 pack_swap(f32x4 a, f32x4 b) {
  auto s0 = __builtin_amdgcn_permlane16_swap(pk2(a[0], a[1]), pk2(b[0], b[1]), false, false);
  auto s1 = __builtin_amdgcn_permlane16_swap(pk2(a[2], a[3]), pk2(b[2], b[3]), false, false);
  return (u32x4){s0[0], s1[0], s0[1], s1[1]};
}
DI void poll(const GAS u64* f, int n, u64 tag, int lane) {
  const GAS u64* p = f + (lane < n ? lane : 0);
  for (unsigned s = 0; s < SPIN_CAP; ++s) {
    if (__all(__hip_atomic_load(p, RLX_AGENT) == tag)) break;
    __builtin_amdgcn_s_sleep(2);
  }
  __builtin_amdgcn_fence(__ATOMIC_ACQUIRE, "wavefront");
}
DI void arrive(unsigned cnt, GAS u64* flag, u64 tag, int lane) {
  wait_vm<0>();
  if (lane == 0) {
    unsigned old = __hip_atomic_fetch_add((LAS unsigned*)cnt, 1u, __ATOMIC_RELAXED, __HIP_MEMORY_SCOPE_WORKGROUP);
    if (old == 7) __hip_atomic_store(flag, tag, RLX_AGENT);
  }
}

DI f32x16 safe_tile(bf16x8 q, const char* kvhead, int kt_hi, int kt_lo, int qpos, bool windowed, int lane) {
  const __amdgpu_buffer_rsrc_t srd = mksrd(kvhead, 131072);
  const int h = lane >> 5, r = lane & 31;
  const unsigned ones = (r == 16 || r == 20) ? 0x3f803f80u : 0u;
  f32x16 o; for (int i = 0; i < 16; ++i) o[i] = 0.f;
  float m = -__builtin_inff();
#pragma unroll 1
  for (int kt = kt_hi; kt >= kt_lo; --kt) {
    u32x4 kf = __builtin_amdgcn_raw_buffer_load_b128(srd, lane * 16, kt * 2048, 16);
    u32x4 v0 = __builtin_amdgcn_raw_buffer_load_b128(srd, 1024 + h * 256 + (r & 15) * 16, kt * 2048, 16);
    u32x4 v1 = __builtin_amdgcn_raw_buffer_load_b128(srd, 1536 + h * 256 + (r & 15) * 16, kt * 2048, 16);
    if (r >= 16) { v0 = (u32x4){ones, ones, ones, ones}; v1 = v0; }
    f32x16 z; for (int i = 0; i < 16; ++i) z[i] = 0.f;
    f32x16 s = __builtin_amdgcn_mfma_f32_32x32x16_bf16(__builtin_bit_cast(bf16x8, kf), q, z, 0, 0, 0);
    float mx = m;
    for (int i = 0; i < 16; ++i) {
      const int dlt = qpos - (kt * 32 + 16 * h + i);
      if (windowed && (dlt < 0 || dlt >= 256)) s[i] = -__builtin_inff();
      mx = fmaxf(mx, s[i]);
    }
    mx = fmaxf(mx, __shfl_xor(mx, 32));
    const float alpha = __builtin_amdgcn_exp2f(m - mx);
    m = mx;
    for (int i = 0; i < 16; ++i) { o[i] *= alpha; s[i] = __builtin_amdgcn_exp2f(s[i] - mx); }
    f32x8 p0 = __builtin_shufflevector(s, s, 0, 1, 2, 3, 4, 5, 6, 7), p1 = __builtin_shufflevector(s, s, 8, 9, 10, 11, 12, 13, 14, 15);
    o = __builtin_amdgcn_smfmac_f32_32x32x32_bf16(__builtin_bit_cast(bf16x8, v0), __builtin_bit_cast(bf16x16, p0), o, 0xdddd, 0, 0);
    o = __builtin_amdgcn_smfmac_f32_32x32x32_bf16(__builtin_bit_cast(bf16x8, v1), __builtin_bit_cast(bf16x16, p1), o, 0xdddd, 0, 0);
  }
  return o;
}
DI bool in_range(const f32x16& o) {
  bool ok = o[8] >= 0x1p-40f && o[8] <= 0x1p90f;
  for (int i = 0; i < 8; ++i) ok = ok && __builtin_fabsf(o[i]) <= 0x1p126f;
  return ok;
}

struct P1 {
  bf16x8 xf[2][16], wf[2][4];
  f32x4 acc[2];
  u32x4 qa, qb;
};

template <int C>
DI void p1_step(P1& st, bool loader, int w, int lane, unsigned wrd, unsigned vE, unsigned vO, __amdgpu_buffer_rsrc_t srdA, __amdgpu_buffer_rsrc_t srdB,
                __amdgpu_buffer_rsrc_t srdKV, unsigned kvo_k, unsigned kvo_v, unsigned kv_tile, GAS u64* fkv, u64 tag) {
  if constexpr (C < 24) { constexpr int last = C == 0 ? 3 : (C + 6 > 23 ? 23 : C + 6); if (loader) wait_vm<4 * (last - C)>(); }
  if constexpr (C == 12 || C == 24) { if (!loader) wait_vm<0>(); }
  bar();
  if constexpr (C == 12 || C == 24) { if (w == 4 && lane == 0) __hip_atomic_store(fkv + (C == 24 ? 256 : 0), tag, RLX_AGENT); }
  if (loader) {
    constexpr int c0 = C == 0 ? 4 : C + 7, c1 = C == 0 ? 7 : C + 7;
#pragma unroll
    for (int c = c0; c <= c1; ++c) if (c < 24) {
      const int t = (c >> 2) % 3, col = t == 0 ? 1024 : (t == 1 ? 2048 : 0);
#pragma unroll
      for (int i = 0; i < 4; ++i)
        dma1(((c + 4) & 7) * 16384 + w * 4096 + i * 1024, (i & 1) ? vO : vE, c < 12 ? srdA : srdB, col + ((c & 3) * 128 + i * 8) * 3072 + w * (32 * 3072));
    }
  }
  if constexpr (C < 24) {
#pragma unroll
    for (int ks = 0; ks < 4; ++ks) st.wf[C & 1][ks] = lds_tr(wrd + ((C + 4) & 7) * 16384 + ks * 4096, 512);
  }
  if constexpr (C >= 5 && (C - 1) % 4 == 0 && ((C - 5) >> 2) % 3 != 2) {
    constexpr int T = (C - 5) >> 2, isv = T % 3;
    if (!loader) {
      u32x4 f = lds_ld16(L_HAND + (w - 4) * 2048 + isv * 1024 + lane * 16);
      __builtin_amdgcn_raw_buffer_store_b128(f, srdKV, isv ? kvo_v : kvo_k, kv_tile - 2 * 131072 + (T / 3) * (4u << 20), 16);
    }
  }
  if constexpr (C >= 1) {
    constexpr int P = C - 1, T = P >> 2, ty = T % 3;
    if constexpr ((P & 3) == 0) { st.acc[0] = (f32x4){0.f, 0.f, 0.f, 0.f}; st.acc[1] = st.acc[0]; }
#pragma unroll
    for (int ks = 0; ks < 4; ++ks)
#pragma unroll
      for (int u = 0; u < 2; ++u) {
        if constexpr (ty == 1) st.acc[u] = mfma16(st.xf[u][(P & 3) * 4 + ks], st.wf[P & 1][ks], st.acc[u]);
        else st.acc[u] = mfma16(st.wf[P & 1][ks], st.xf[u][(P & 3) * 4 + ks], st.acc[u]);
      }
    if constexpr ((P & 3) == 3) {
      if constexpr (ty == 2) {
        u32x4 f = pack_swap(st.acc[0] * QSCALE, st.acc[1] * QSCALE);
        if constexpr (T == 2) st.qa = f; else st.qb = f;
      } else {
        u32x4 f;
        if constexpr (ty == 0) f = pack_swap(st.acc[0], st.acc[1]);
        else f = (u32x4){pk2(st.acc[0][0], st.acc[0][1]), pk2(st.acc[0][2], st.acc[0][3]), pk2(st.acc[1][0], st.acc[1][1]), pk2(st.acc[1][2], st.acc[1][3])};
        if (loader) lds_st16(L_HAND + w * 2048 + ty * 1024 + lane * 16, f);
        else __builtin_amdgcn_raw_buffer_store_b128(f, srdKV, ty ? kvo_v : kvo_k, kv_tile + (T / 3) * (4u << 20), 16);
      }
    }
  }
  if constexpr (C < 24) p1_step<C + 1>(st, loader, w, lane, wrd, vE, vO, srdA, srdB, srdKV, kvo_k, kvo_v, kv_tile, fkv, tag);
}

__global__ void __launch_bounds__(512, 2)
gated_mix_kernel(const char* __restrict__ x, const char* __restrict__ wqkvA, const char* __restrict__ woA, const char* __restrict__ wqkvB,
                 const char* __restrict__ woB, const char* __restrict__ gatew, char* __restrict__ out, char* ws) {
  extern __shared__ __attribute__((aligned(16))) char smem[];
  const int lane = threadIdx.x & 63, w = __builtin_amdgcn_readfirstlane(threadIdx.x >> 6);
  const int xcd = blockIdx.x & 7, bi = blockIdx.x >> 3;
  const int j = xcd, tb = bi;
  const int hh = w >> 1, th = w & 1, head = 4 * j + hh;
  const bool loader = w < 4;
  STAMP(0);
  const __amdgpu_buffer_rsrc_t srdA = mksrd(wqkvA + j * 128, 512 * 3072 - j * 128), srdB = mksrd(wqkvB + j * 128, 512 * 3072 - j * 128);
  const unsigned vE = (lane >> 3) * 3072 + (((lane & 7) ^ (((lane >> 4) & 1) << 1)) << 4), vO = vE ^ 64;
  if (loader) {
#pragma unroll
    for (int c = 0; c < 4; ++c)
#pragma unroll
      for (int i = 0; i < 4; ++i) dma1((c + 4) * 16384 + w * 4096 + i * 1024, (i & 1) ? vO : vE, srdA, 1024 + (c * 128 + i * 8) * 3072 + w * (32 * 3072));
  } else {
    const __amdgpu_buffer_rsrc_t srdX = mksrd(x + tb * 65536, 65536);
#pragma unroll
    for (int i = 0; i < 16; ++i) dma1(((w - 4) * 16 + i) * 1024, (lane ^ i) << 4, srdX, ((w - 4) * 16 + i) * 1024);
    if (w == 4) dma1(L_GATEW, lane << 4, mksrd(gatew, 1024), 0);
  }
  u64 tag;
  asm volatile("s_load_dwordx2 %0, %1, 0x0\n\ts_waitcnt lgkmcnt(0)" : "=s"(tag) : "s"(ws + WS_EPOCH) : "memory");
  tag = tag * 0x9E3779B97F4A7C15ull + 0xD1B54A32D192ED03ull;
  if (threadIdx.x < 16) *(LAS unsigned*)(L_CNT + threadIdx.x * 4) = 0u;
  if (threadIdx.x < 128) *(LAS float*)(L_MTAB + threadIdx.x * 4) = ((threadIdx.x >> 5) == 1 || (threadIdx.x >> 5) == 2) ? -__builtin_inff() : 0.f;
  if (!loader) wait_vm<0>();
  bar();
  STAMP(1);
  P1 st;
  {
    const int c = lane & 15, g = lane >> 4;
#pragma unroll
    for (int s = 0; s < 16; ++s)
#pragma unroll
      for (int u = 0; u < 2; ++u)
        st.xf[u][s] = __builtin_bit_cast(bf16x8, lds_ld16((th * 32 + c) * 1024 + (((4 * (s & 3) + g) ^ c) << 4) + u * 16384 + (s >> 2) * 256));
    f32x4 ga[2] = {{0.f, 0.f, 0.f, 0.f}, {0.f, 0.f, 0.f, 0.f}};
#pragma unroll
    for (int s = 0; s < 4; ++s) {
      const bf16x8 gw = __builtin_bit_cast(bf16x8, lds_ld16(L_GATEW + hh * 256 + s * 64 + g * 16));
#pragma unroll
      for (int u = 0; u < 2; ++u) {
        const bf16x8 xx = __builtin_bit_cast(bf16x8, lds_ld16((th * 32 + c) * 1024 + (((4 * s + g) ^ c) << 4) + u * 16384 + hh * 256));
        ga[u] = mfma16(gw, xx, ga[u]);
      }
    }
    if (lane < 32) *(LAS float*)(L_GPART + hh * 256 + th * 128 + lane * 4) = g ? ga[1][0] : ga[0][0];
  }
  const __amdgpu_buffer_rsrc_t srdKV = mksrd(ws + WS_KV, 8u << 20);
  {
    const int g = lane >> 4, q = (lane >> 2) & 3, p = lane & 3, t = lane & 31;
    const unsigned wrd = (8 * g + q) * 128 + ((hh ^ ((q >> 1) | ((g & 1) << 1))) << 5) + p * 8;
    const unsigned kvo_k = ((lane & 32) + ((t >> 2) & 3) * 8 + (t >> 4) * 4 + (t & 3)) * 16, kvo_v = 1024 + lane * 16;
    p1_step<0>(st, loader, w, lane, wrd, vE, vO, srdA, srdB, srdKV, kvo_k, kvo_v, head * 131072 + (2 * tb + th) * 2048,
               (GAS u64*)(ws + WS_FKV) + j * 32 + tb, tag);
  }
  lds_st16(L_COMB + w * 1024 + lane * 16, st.qa);
  bar();
  STAMP(2);
  const u32x4 qo = lds_ld16(L_COMB + (w ^ 1) * 1024 + lane * 16);
  float gate;
  {
    const unsigned a = L_GPART + th * 128 + (lane & 31) * 4;
    const float z = (*(LAS float*)a + *(LAS float*)(a + 256)) + (*(LAS float*)(a + 512) + *(LAS float*)(a + 768));
    gate = 1.f / (1.f + __expf(-z));
  }
  const int r = lane & 31, h = lane >> 5;
  const unsigned vk = lane * 16, vv = h * 256 + (r & 15) * 16, idx = 0xddddu;
  u32x32 vring; { const unsigned ones = (r == 16 || r == 20) ? 0x3f803f80u : 0u; for (int i = 0; i < 32; ++i) vring[i] = ones; }
  f32x16 o0, o1; for (int i = 0; i < 16; ++i) { o0[i] = 0.f; o1[i] = 0.f; }
  const char* kvA = ws + WS_KV + head * 131072; const char* kvB = kvA + (4u << 20);
  const __amdgpu_buffer_rsrc_t srdO = mksrd(ws + WS_O, 4u << 20);
  const int qt = 2 * tb + th;
  const unsigned ovo = (qt * 2 + (r >> 4)) * 16384 + (head >> 1) * 1024 + ((head & 1) * 2 + h) * 256 + (r & 15) * 16;
  const int tb3 = xcd * 4 + (bi >> 3), cb3 = bi & 7;
  __builtin_amdgcn_s_setprio(3);
  bar();
  poll((const GAS u64*)(ws + WS_FKV) + j * 32 + th * 16, 16, tag, lane);
  STAMP(3);
#pragma unroll 1
  for (int part = 0; part < 2; ++part) {
    const u64 base = (u64)(kvA + th * 65536);
    asm volatile(ATTN_A_ASM : [o0] "+v"(o0), [o1] "+v"(o1), "+{v[192:223]}"(vring)
                 : [q0] "v"(st.qa), [q1] "v"(qo), [idx] "v"(idx), [vk] "v"(vk), [vv] "v"(vv), [b0] "s"((unsigned)base), [b1] "s"((unsigned)(base >> 32) & 0xffffu),
                   [nrec] "s"(65536u), [so] "s"(((tb & 15) * 4096 + part * 32768) & 0xffff), [fin] "s"(part) : ATTN_CLOBBERS);
    if (part == 0) {
      STAMP(4);
      const int lo = tb >= 4 ? tb - 4 : 0;
      poll((const GAS u64*)(ws + WS_FKV) + 256 + j * 32 + lo, tb - lo + 1, tag, lane);
      f32x16 md, mf, ob;
      const unsigned ma = L_MTAB + (31 - r + 16 * h) * 4;
      for (int i = 0; i < 16; ++i) { md[i] = *(LAS float*)(ma + i * 4); mf[i] = *(LAS float*)(ma + 256 + i * 4); ob[i] = 0.f; }
      const u64 bb = (u64)kvB;
      asm volatile(ATTN_B_ASM : [o0] "+v"(ob), "+{v[192:223]}"(vring)
                   : [q0] "v"(st.qb), [idx] "v"(idx), [vk] "v"(vk), [vv] "v"(vv), [b0] "s"((unsigned)bb), [b1] "s"((unsigned)(bb >> 32) & 0xffffu),
                     [nrec] "s"(131072u), [so] "s"(qt * 2048), [nt] "s"(qt + 1), [md] "v"(md), [mf] "v"(mf) : ATTN_CLOBBERS);
      if (__builtin_expect(!__all(in_range(ob)), 0))
        ob = safe_tile(__builtin_bit_cast(bf16x8, st.qb), kvB, qt, qt >= 8 ? qt - 8 : 0, qt * 32 + r, true, lane);
      const float sc = (1.f - gate) / ob[8];
      __builtin_amdgcn_raw_buffer_store_b128((u32x4){pk2(ob[0] * sc, ob[1] * sc), pk2(ob[2] * sc, ob[3] * sc), pk2(ob[4] * sc, ob[5] * sc), pk2(ob[6] * sc, ob[7] * sc)},
                                             srdO, ovo, 2u << 20, 16);
      const __amdgpu_buffer_rsrc_t srdW = mksrd((w < 4 ? woA : woB) + cb3 * 128, 512 * 1024 - cb3 * 128);
      const unsigned vW = (lane >> 3) * 1024 + (((lane & 7) ^ (((lane >> 4) & 3) << 1)) << 4);
#pragma unroll
      for (int i = 0; i < 16; ++i) dma1(w * 16384 + i * 1024, vW, srdW, ((w & 3) * 128 + i * 8) * 1024);
      arrive(L_CNT, (GAS u64*)(ws + WS_FO) + 512 + tb * 16 + j, tag, lane);
      __builtin_amdgcn_s_setprio(2);
      STAMP(5);
    }
  }
  STAMP(6);
  {
    const unsigned cw = L_COMB + w * 2304, cr = L_COMB + (w ^ 1) * 2304;
    lds_st16(cw + lane * 16, __builtin_bit_cast(u32x4, __builtin_shufflevector(o1, o1, 0, 1, 2, 3)));
    lds_st16(cw + 1024 + lane * 16, __builtin_bit_cast(u32x4, __builtin_shufflevector(o1, o1, 4, 5, 6, 7)));
    *(LAS float*)(cw + 2048 + lane * 4) = o1[8];
    asm volatile("s_waitcnt lgkmcnt(0)" ::: "memory");
    __hip_atomic_store((LAS unsigned*)(L_CNT + 16 + w * 4), 1u, __ATOMIC_RELAXED, __HIP_MEMORY_SCOPE_WORKGROUP);
    for (unsigned s = 0; s < SPIN_CAP; ++s) {
      if (__builtin_amdgcn_readfirstlane(__hip_atomic_load((LAS unsigned*)(L_CNT + 16 + (w ^ 1) * 4), __ATOMIC_RELAXED, __HIP_MEMORY_SCOPE_WORKGROUP))) break;
      __builtin_amdgcn_s_sleep(1);
    }
    asm volatile("" ::: "memory");
    const f32x4 a = __builtin_bit_cast(f32x4, lds_ld16(cr + lane * 16)), b = __builtin_bit_cast(f32x4, lds_ld16(cr + 1024 + lane * 16));
    for (int i = 0; i < 4; ++i) { o0[i] += a[i]; o0[4 + i] += b[i]; }
    o0[8] += *(LAS float*)(cr + 2048 + lane * 4);
    if (__builtin_expect(!__all(in_range(o0)), 0)) o0 = safe_tile(__builtin_bit_cast(bf16x8, st.qa), kvA, 63, 0, 0, false, lane);
    const float sc = gate / o0[8];
    __builtin_amdgcn_raw_buffer_store_b128((u32x4){pk2(o0[0] * sc, o0[1] * sc), pk2(o0[2] * sc, o0[3] * sc), pk2(o0[4] * sc, o0[5] * sc), pk2(o0[6] * sc, o0[7] * sc)},
                                           srdO, ovo, 0, 16);
    arrive(L_CNT + 4, (GAS u64*)(ws + WS_FO) + tb * 16 + j, tag, lane);
  }
  STAMP(7);
  bar();
  {
    const int ts = w & 3, kh = w >> 2, g = lane >> 4, q = (lane >> 2) & 3, p = lane & 3;
    const unsigned wrd = (16 * (g >> 1) + 4 * (g & 1) + q) * 128 + ((((g & 1) << 1) | (q >> 1)) << 5) + p * 8 + kh * 32768;
    f32x4 acc[4]; for (int n = 0; n < 4; ++n) acc[n] = (f32x4){0.f, 0.f, 0.f, 0.f};
#pragma unroll 1
    for (int br = 1; br >= 0; --br) {
      poll((const GAS u64*)(ws + WS_FO) + br * 512 + tb3 * 16, 8, tag, lane);
      STAMP(8 + br);
      u32x4 of[8];
#pragma unroll
      for (int m = 0; m < 8; ++m) of[m] = __builtin_amdgcn_raw_buffer_load_b128(srdO, lane * 16 + m * 1024, br * (2u << 20) + (tb3 * 4 + ts) * 16384 + kh * 8192, 16);
#pragma unroll
      for (int m = 0; m < 8; ++m)
#pragma unroll
        for (int n = 0; n < 4; ++n) acc[n] = mfma16(lds_tr(((wrd ^ (n << 5)) + br * 65536) + m * 4096, 1024), __builtin_bit_cast(bf16x8, of[m]), acc[n]);
    }
    if (blockIdx.x == 0 && threadIdx.x == 0) __hip_atomic_store((GAS u64*)(ws + WS_EPOCH), tag, RLX_AGENT);
    lds_st16(L_COMB + w * 2048 + lane * 16, __builtin_bit_cast(u32x4, kh ? acc[0] : acc[2]));
    lds_st16(L_COMB + w * 2048 + 1024 + lane * 16, __builtin_bit_cast(u32x4, kh ? acc[1] : acc[3]));
    bar();
    const f32x4 a = (kh ? acc[2] : acc[0]) + __builtin_bit_cast(f32x4, lds_ld16(L_COMB + (w ^ 4) * 2048 + lane * 16));
    const f32x4 b = (kh ? acc[3] : acc[1]) + __builtin_bit_cast(f32x4, lds_ld16(L_COMB + (w ^ 4) * 2048 + 1024 + lane * 16));
    *(GAS u32x4*)(out + (size_t)(tb3 * 64 + ts * 16 + (lane & 15)) * 1024 + cb3 * 128 + kh * 64 + (g & 1) * 32 + (g >> 1) * 16) = pack_swap(a, b);
  }
  STAMP(10);
}

extern "C" void kernel_launch(void* const* d_in, const int* in_sizes, int n_in, void* d_out, int out_size, void* d_ws, size_t ws_size, hipStream_t stream) {
  (void)in_sizes; (void)n_in; (void)out_size; (void)ws_size;
  (void)hipFuncSetAttribute((const void*)gated_mix_kernel, hipFuncAttributeMaxDynamicSharedMemorySize, L_TOTAL);
  gated_mix_kernel<<<256, 512, L_TOTAL, stream>>>((const char*)d_in[0], (const char*)d_in[1], (const char*)d_in[2], (const char*)d_in[4],
                                                  (const char*)d_in[5], (const char*)d_in[6], (char*)d_out, (char*)d_ws);
}
```

```cpp
#include <hip/hip_runtime.h>
#include <stdint.h>

typedef __attribute__((ext_vector_type(8))) __bf16 bf16x8;
typedef __attribute__((ext_vector_type(16))) __bf16 bf16x16;
typedef __attribute__((ext_vector_type(8))) float f32x8;
typedef __attribute__((ext_vector_type(16))) float f32x16;
typedef __attribute__((ext_vector_type(2))) unsigned u32x2;
typedef __attribute__((ext_vector_type(4))) unsigned u32x4;
typedef __attribute__((ext_vector_type(8))) unsigned u32x8;
typedef __attribute__((ext_vector_type(16))) unsigned u32x16;
typedef __attribute__((ext_vector_type(32))) unsigned u32x32;
typedef unsigned long long u64;
#define LAS __attribute__((address_space(3)))
#define GAS __attribute__((address_space(1)))
#define DI __device__ __forceinline__

#define PART1_ASM \
      \
    "s_and_b32 s42, s64, 0xffff0000\n\t" \
    "s_mov_b32 s43, s65\n\t" \
    "s_load_dwordx2 s[36:37], s[42:43], 0x0\n\t" \
    "s_lshl_b32 s47, s74, 11\n\t" \
    "s_mul_hi_u32 s51, s88, 2796203\n\t" \
    "s_add_u32 m0, s47, 32768\n\t" \
    "s_add_u32 s49, s88, 2048\n\t" \
    "buffer_load_dwordx4 v2, s[80:83], s49 offen lds\n\t" \
    "buffer_load_dwordx4 v3, s[80:83], s49 offen offset:1024 lds\n\t" \
    "s_add_u32 m0, s47, 49152\n\t" \
    "s_add_u32 s49, s88, 0\n\t" \
    "buffer_load_dwordx4 v2, s[80:83], s49 offen lds\n\t" \
    "buffer_load_dwordx4 v3, s[80:83], s49 offen offset:1024 lds\n\t" \
    "v_xor_b32 v20, 64, v4\n\t" \
    "v_xor_b32 v21, 128, v4\n\t" \
    "v_xor_b32 v22, 192, v4\n\t" \
    "s_mov_b32 s41, s71\n\t" \
    "s_mov_b32 s44, 0xffff\n\t" \
    "s_mov_b32 s45, 0xffff\n\t" \
    "s_mov_b32 s40, s70\n\t" \
    "s_mov_b32 s48, 16\n\t" \
    "v_mov_b32 v192, v8\n\t" \
    "v_mov_b32 v193, v8\n\t" \
    "v_mov_b32 v194, v8\n\t" \
    "v_mov_b32 v195, v8\n\t" \
    "v_mov_b32 v196, v8\n\t" \
    "v_mov_b32 v197, v8\n\t" \
    "v_mov_b32 v198, v8\n\t" \
    "v_mov_b32 v199, v8\n\t" \
    "v_mov_b32 v200, v8\n\t" \
    "v_mov_b32 v201, v8\n\t" \
    "v_mov_b32 v202, v8\n\t" \
    "v_mov_b32 v203, v8\n\t" \
    "v_mov_b32 v204, v8\n\t" \
    "v_mov_b32 v205, v8\n\t" \
    "v_mov_b32 v206, v8\n\t" \
    "v_mov_b32 v207, v8\n\t" \
    "v_mov_b32 v208, v8\n\t" \
    "v_mov_b32 v209, v8\n\t" \
    "v_mov_b32 v210, v8\n\t" \
    "v_mov_b32 v211, v8\n\t" \
    "v_mov_b32 v212, v8\n\t" \
    "v_mov_b32 v213, v8\n\t" \
    "v_mov_b32 v214, v8\n\t" \
    "v_mov_b32 v215, v8\n\t" \
    "v_mov_b32 v216, v8\n\t" \
    "v_mov_b32 v217, v8\n\t" \
    "v_mov_b32 v218, v8\n\t" \
    "v_mov_b32 v219, v8\n\t" \
    "v_mov_b32 v220, v8\n\t" \
    "v_mov_b32 v221, v8\n\t" \
    "v_mov_b32 v222, v8\n\t" \
    "v_mov_b32 v223, v8\n\t" \
    "v_mov_b32 v8, 0xdddd\n\t" \
      \
    "s_waitcnt vmcnt(4) lgkmcnt(0)\n\t" \
    "s_barrier\n\t" \
    "s_add_u32 m0, s47, 65536\n\t" \
    "s_add_u32 s49, s51, 256\n\t" \
    "s_and_b32 s49, s49, 0x3ff\n\t" \
    "buffer_load_dwordx4 v11, s[84:87], s49 offen lds\n\t" \
    "buffer_load_dwordx4 v5, s[84:87], s49 offen offset:1024 lds\n\t" \
    "s_add_u32 m0, s47, 81920\n\t" \
    "s_add_u32 s49, s89, 1024\n\t" \
    "buffer_load_dwordx4 v2, s[80:83], s49 offen lds\n\t" \
    "buffer_load_dwordx4 v3, s[80:83], s49 offen offset:1024 lds\n\t" \
    "ds_read_b128 v[176:179], v12 offset:0\n\t" \
    "ds_read_b128 v[180:183], v12 offset:64\n\t" \
    "ds_read_b128 v[184:187], v12 offset:128\n\t" \
    "s_mul_hi_u32 s50, s36, 0x7f4a7c15\n\t" \
    "ds_read_b128 v[188:191], v12 offset:192\n\t" \
    "ds_read_b128 v[48:51], v4 offset:0\n\t" \
    "ds_read_b128 v[52:55], v4 offset:4096\n\t" \
    "s_mul_i32 s37, s37, 0x7f4a7c15\n\t" \
    "ds_read_b64_tr_b16 v[224:225], v0 offset:16384\n\t" \
    "ds_read_b64_tr_b16 v[226:227], v0 offset:16896\n\t" \
    "ds_read_b128 v[56:59], v20 offset:0\n\t" \
    "s_add_u32 s37, s37, s50\n\t" \
    "ds_read_b128 v[60:63], v20 offset:4096\n\t" \
    "ds_read_b64_tr_b16 v[228:229], v0 offset:20480\n\t" \
    "ds_read_b64_tr_b16 v[230:231], v0 offset:20992\n\t" \
    "s_mul_i32 s50, s36, 0x9e3779b9\n\t" \
    "ds_read_b128 v[64:67], v21 offset:0\n\t" \
    "ds_read_b128 v[68:71], v21 offset:4096\n\t" \
    "ds_read_b64_tr_b16 v[232:233], v0 offset:24576\n\t" \
    "s_add_u32 s37, s37, s50\n\t" \
    "ds_read_b64_tr_b16 v[234:235], v0 offset:25088\n\t" \
    "ds_read_b128 v[72:75], v22 offset:0\n\t" \
    "ds_read_b128 v[76:79], v22 offset:4096\n\t" \
    "s_mul_i32 s36, s36, 0x7f4a7c15\n\t" \
    "ds_read_b64_tr_b16 v[236:237], v0 offset:28672\n\t" \
    "ds_read_b64_tr_b16 v[238:239], v0 offset:29184\n\t" \
    "s_add_u32 s36, s36, 0xd192ed03\n\t" \
    "s_addc_u32 s37, s37, 0xd1b54a32\n\t" \
    "s_bfe_u32 s50, s60, 0x5000c\n\t" \
    "s_lshl_b32 s50, s50, 3\n\t" \
    "s_add_u32 s50, s50, s74\n\t" \
    "s_mulk_i32 s50, 6144\n\t" \
    "v_lshlrev_b32 v28, 6, v9\n\t" \
    "s_mov_b64 exec, 63\n\t" \
    "buffer_load_dword v28, v28, s[52:55], s50 offen\n\t" \
    "s_mov_b64 exec, -1\n\t" \
      \
    "s_waitcnt vmcnt(7)\n\t" \
    "s_barrier\n\t" \
    "s_waitcnt lgkmcnt(12)\n\t" \
    "v_mfma_f32_16x16x32_bf16 v[32:35], v[224:227], v[48:51], 0\n\t" \
    "ds_read_b64_tr_b16 v[240:241], v0 offset:32768\n\t" \
    "v_lshrrev_b32 v12, 3, v9\n\t" \
    "v_mfma_f32_16x16x32_bf16 v[36:39], v[224:227], v[52:55], 0\n\t" \
    "ds_read_b64_tr_b16 v[242:243], v0 offset:33280\n\t" \
    "v_and_b32 v12, 0x60, v12\n\t" \
    "s_waitcnt lgkmcnt(10)\n\t" \
    "v_mfma_f32_16x16x32_bf16 v[32:35], v[228:231], v[56:59], v[32:35]\n\t" \
    "ds_read_b64_tr_b16 v[244:245], v0 offset:36864\n\t" \
    "s_add_u32 m0, s47, 98304\n\t" \
    "s_add_u32 s49, s89, 2048\n\t" \
    "buffer_load_dwordx4 v2, s[80:83], s49 offen lds\n\t" \
    "v_xor_b32 v12, v12, v9\n\t" \
    "v_mfma_f32_16x16x32_bf16 v[36:39], v[228:231], v[60:63], v[36:39]\n\t" \
    "ds_read_b64_tr_b16 v[246:247], v0 offset:37376\n\t" \
    "v_and_b32 v12, 0x70, v12\n\t" \
    "s_waitcnt lgkmcnt(8)\n\t" \
    "v_mfma_f32_16x16x32_bf16 v[32:35], v[232:235], v[64:67], v[32:35]\n\t" \
    "ds_read_b64_tr_b16 v[248:249], v0 offset:40960\n\t" \
    "v_lshrrev_b32 v23, 7, v9\n\t" \
    "v_mfma_f32_16x16x32_bf16 v[36:39], v[232:235], v[68:71], v[36:39]\n\t" \
    "ds_read_b64_tr_b16 v[250:251], v0 offset:41472\n\t" \
    "v_lshl_or_b32 v12, v23, 10, v12\n\t" \
    "s_waitcnt lgkmcnt(6)\n\t" \
    "v_mfma_f32_16x16x32_bf16 v[32:35], v[236:239], v[72:75], v[32:35]\n\t" \
    "ds_read_b64_tr_b16 v[252:253], v0 offset:45056\n\t" \
    "buffer_load_dwordx4 v3, s[80:83], s49 offen offset:1024 lds\n\t" \
    "v_mfma_f32_16x16x32_bf16 v[36:39], v[236:239], v[76:79], v[36:39]\n\t" \
    "ds_read_b64_tr_b16 v[254:255], v0 offset:45568\n\t" \
    "s_lshr_b32 s50, s74, 1\n\t" \
    "s_cmp_lg_u32 s50, 0\n\t" \
    "s_cbranch_scc1 102f\n\t" \
    "v_mfma_f32_16x16x32_bf16 v[16:19], v[176:179], v[48:51], 0\n\t" \
    "v_mfma_f32_16x16x32_bf16 v[24:27], v[176:179], v[52:55], 0\n\t" \
    "v_mfma_f32_16x16x32_bf16 v[16:19], v[180:183], v[56:59], v[16:19]\n\t" \
    "v_mfma_f32_16x16x32_bf16 v[24:27], v[180:183], v[60:63], v[24:27]\n\t" \
    "v_mfma_f32_16x16x32_bf16 v[16:19], v[184:187], v[64:67], v[16:19]\n\t" \
    "v_mfma_f32_16x16x32_bf16 v[24:27], v[184:187], v[68:71], v[24:27]\n\t" \
    "v_mfma_f32_16x16x32_bf16 v[16:19], v[188:191], v[72:75], v[16:19]\n\t" \
    "v_mfma_f32_16x16x32_bf16 v[24:27], v[188:191], v[76:79], v[24:27]\n\t" \
    "102:\n\t" \
      \
    "s_waitcnt vmcnt(7)\n\t" \
    "s_barrier\n\t" \
    "s_waitcnt lgkmcnt(4)\n\t" \
    "v_mfma_f32_16x16x32_bf16 v[40:43], v[48:51], v[240:243], 0\n\t" \
    "ds_read_b64_tr_b16 v[224:225], v0 offset:49152\n\t" \
    "v_mfma_f32_16x16x32_bf16 v[44:47], v[52:55], v[240:243], 0\n\t" \
    "ds_read_b64_tr_b16 v[226:227], v0 offset:49664\n\t" \
    "v_mfma_f32_16x16x32_bf16 v[40:43], v[56:59], v[244:247], v[40:43]\n\t" \
    "ds_read_b64_tr_b16 v[228:229], v0 offset:53248\n\t" \
    "s_add_u32 m0, s47, 114688\n\t" \
    "s_add_u32 s49, s89, 0\n\t" \
    "buffer_load_dwordx4 v2, s[80:83], s49 offen lds\n\t" \
    "v_mfma_f32_16x16x32_bf16 v[44:47], v[60:63], v[244:247], v[44:47]\n\t" \
    "ds_read_b64_tr_b16 v[230:231], v0 offset:53760\n\t" \
    "s_waitcnt lgkmcnt(4)\n\t" \
    "v_mfma_f32_16x16x32_bf16 v[40:43], v[64:67], v[248:251], v[40:43]\n\t" \
    "ds_read_b64_tr_b16 v[232:233], v0 offset:57344\n\t" \
    "v_mfma_f32_16x16x32_bf16 v[44:47], v[68:71], v[248:251], v[44:47]\n\t" \
    "ds_read_b64_tr_b16 v[234:235], v0 offset:57856\n\t" \
    "v_mfma_f32_16x16x32_bf16 v[40:43], v[72:75], v[252:255], v[40:43]\n\t" \
    "ds_read_b64_tr_b16 v[236:237], v0 offset:61440\n\t" \
    "buffer_load_dwordx4 v3, s[80:83], s49 offen offset:1024 lds\n\t" \
    "v_mfma_f32_16x16x32_bf16 v[44:47], v[76:79], v[252:255], v[44:47]\n\t" \
    "ds_read_b64_tr_b16 v[238:239], v0 offset:61952\n\t" \
      \
    "s_waitcnt vmcnt(5)\n\t" \
    "s_barrier\n\t" \
    "v_xor_b32 v4, 0x10000, v4\n\t" \
    "v_xor_b32 v20, 0x10000, v20\n\t" \
    "v_xor_b32 v21, 0x10000, v21\n\t" \
    "v_xor_b32 v22, 0x10000, v22\n\t" \
    "s_waitcnt lgkmcnt(6)\n\t" \
    "v_mfma_f32_16x16x32_bf16 v[216:219], v[224:227], v[48:51], 0\n\t" \
    "ds_read_b128 v[80:83], v4 offset:0\n\t" \
    "ds_read_b128 v[84:87], v4 offset:4096\n\t" \
    "v_mfma_f32_16x16x32_bf16 v[220:223], v[224:227], v[52:55], 0\n\t" \
    "ds_read_b64_tr_b16 v[240:241], v1 offset:16384\n\t" \
    "ds_read_b64_tr_b16 v[242:243], v1 offset:16896\n\t" \
    "s_add_u32 m0, s47, 0\n\t" \
    "s_add_u32 s49, s51, 512\n\t" \
    "s_and_b32 s49, s49, 0x3ff\n\t" \
    "buffer_load_dwordx4 v11, s[84:87], s49 offen lds\n\t" \
    "s_waitcnt lgkmcnt(8)\n\t" \
    "v_mfma_f32_16x16x32_bf16 v[216:219], v[228:231], v[56:59], v[216:219]\n\t" \
    "ds_read_b128 v[88:91], v20 offset:0\n\t" \
    "ds_read_b128 v[92:95], v20 offset:4096\n\t" \
    "v_mfma_f32_16x16x32_bf16 v[220:223], v[228:231], v[60:63], v[220:223]\n\t" \
    "ds_read_b64_tr_b16 v[244:245], v1 offset:20480\n\t" \
    "ds_read_b64_tr_b16 v[246:247], v1 offset:20992\n\t" \
    "buffer_load_dwordx4 v5, s[84:87], s49 offen offset:1024 lds\n\t" \
    "s_waitcnt lgkmcnt(10)\n\t" \
    "v_mfma_f32_16x16x32_bf16 v[216:219], v[232:235], v[64:67], v[216:219]\n\t" \
    "ds_read_b128 v[96:99], v21 offset:0\n\t" \
    "ds_read_b128 v[100:103], v21 offset:4096\n\t" \
    "v_mfma_f32_16x16x32_bf16 v[220:223], v[232:235], v[68:71], v[220:223]\n\t" \
    "ds_read_b64_tr_b16 v[248:249], v1 offset:24576\n\t" \
    "ds_read_b64_tr_b16 v[250:251], v1 offset:25088\n\t" \
    "s_add_u32 m0, s47, 16384\n\t" \
    "s_add_u32 s49, s90, 1024\n\t" \
    "buffer_load_dwordx4 v2, s[80:83], s49 offen lds\n\t" \
    "s_waitcnt lgkmcnt(12)\n\t" \
    "v_mfma_f32_16x16x32_bf16 v[216:219], v[236:239], v[72:75], v[216:219]\n\t" \
    "ds_read_b128 v[104:107], v22 offset:0\n\t" \
    "ds_read_b128 v[108:111], v22 offset:4096\n\t" \
    "v_mfma_f32_16x16x32_bf16 v[220:223], v[236:239], v[76:79], v[220:223]\n\t" \
    "ds_read_b64_tr_b16 v[252:253], v1 offset:28672\n\t" \
    "ds_read_b64_tr_b16 v[254:255], v1 offset:29184\n\t" \
    "buffer_load_dwordx4 v3, s[80:83], s49 offen offset:1024 lds\n\t" \
      \
    "s_waitcnt vmcnt(6)\n\t" \
    "s_barrier\n\t" \
    "s_waitcnt lgkmcnt(12)\n\t" \
    "v_mfma_f32_16x16x32_bf16 v[32:35], v[240:243], v[80:83], v[32:35]\n\t" \
    "ds_read_b64_tr_b16 v[224:225], v1 offset:32768\n\t" \
    "v_mfma_f32_16x16x32_bf16 v[36:39], v[240:243], v[84:87], v[36:39]\n\t" \
    "ds_read_b64_tr_b16 v[226:227], v1 offset:33280\n\t" \
    "s_waitcnt lgkmcnt(10)\n\t" \
    "v_mfma_f32_16x16x32_bf16 v[32:35], v[244:247], v[88:91], v[32:35]\n\t" \
    "ds_read_b64_tr_b16 v[228:229], v1 offset:36864\n\t" \
    "s_add_u32 m0, s47, 32768\n\t" \
    "s_add_u32 s49, s90, 2048\n\t" \
    "buffer_load_dwordx4 v2, s[80:83], s49 offen lds\n\t" \
    "v_mfma_f32_16x16x32_bf16 v[36:39], v[244:247], v[92:95], v[36:39]\n\t" \
    "ds_read_b64_tr_b16 v[230:231], v1 offset:37376\n\t" \
    "s_waitcnt lgkmcnt(8)\n\t" \
    "v_mfma_f32_16x16x32_bf16 v[32:35], v[248:251], v[96:99], v[32:35]\n\t" \
    "ds_read_b64_tr_b16 v[232:233], v1 offset:40960\n\t" \
    "v_mfma_f32_16x16x32_bf16 v[36:39], v[248:251], v[100:103], v[36:39]\n\t" \
    "ds_read_b64_tr_b16 v[234:235], v1 offset:41472\n\t" \
    "s_waitcnt lgkmcnt(6)\n\t" \
    "v_mfma_f32_16x16x32_bf16 v[32:35], v[252:255], v[104:107], v[32:35]\n\t" \
    "ds_read_b64_tr_b16 v[236:237], v1 offset:45056\n\t" \
    "buffer_load_dwordx4 v3, s[80:83], s49 offen offset:1024 lds\n\t" \
    "v_mfma_f32_16x16x32_bf16 v[36:39], v[252:255], v[108:111], v[36:39]\n\t" \
    "ds_read_b64_tr_b16 v[238:239], v1 offset:45568\n\t" \
    "s_lshr_b32 s50, s74, 1\n\t" \
    "s_cmp_lg_u32 s50, 1\n\t" \
    "s_cbranch_scc1 112f\n\t" \
    "v_mfma_f32_16x16x32_bf16 v[16:19], v[176:179], v[80:83], 0\n\t" \
    "v_mfma_f32_16x16x32_bf16 v[24:27], v[176:179], v[84:87], 0\n\t" \
    "v_mfma_f32_16x16x32_bf16 v[16:19], v[180:183], v[88:91], v[16:19]\n\t" \
    "v_mfma_f32_16x16x32_bf16 v[24:27], v[180:183], v[92:95], v[24:27]\n\t" \
    "v_mfma_f32_16x16x32_bf16 v[16:19], v[184:187], v[96:99], v[16:19]\n\t" \
    "v_mfma_f32_16x16x32_bf16 v[24:27], v[184:187], v[100:103], v[24:27]\n\t" \
    "v_mfma_f32_16x16x32_bf16 v[16:19], v[188:191], v[104:107], v[16:19]\n\t" \
    "v_mfma_f32_16x16x32_bf16 v[24:27], v[188:191], v[108:111], v[24:27]\n\t" \
    "112:\n\t" \
      \
    "s_waitcnt vmcnt(6)\n\t" \
    "s_barrier\n\t" \
    "s_waitcnt lgkmcnt(4)\n\t" \
    "v_mfma_f32_16x16x32_bf16 v[40:43], v[80:83], v[224:227], v[40:43]\n\t" \
    "ds_read_b64_tr_b16 v[240:241], v1 offset:49152\n\t" \
    "v_mfma_f32_16x16x32_bf16 v[44:47], v[84:87], v[224:227], v[44:47]\n\t" \
    "ds_read_b64_tr_b16 v[242:243], v1 offset:49664\n\t" \
    "v_mfma_f32_16x16x32_bf16 v[40:43], v[88:91], v[228:231], v[40:43]\n\t" \
    "ds_read_b64_tr_b16 v[244:245], v1 offset:53248\n\t" \
    "s_add_u32 m0, s47, 49152\n\t" \
    "s_add_u32 s49, s51, 768\n\t" \
    "s_and_b32 s49, s49, 0x3ff\n\t" \
    "buffer_load_dwordx4 v11, s[84:87], s49 offen lds\n\t" \
    "v_mfma_f32_16x16x32_bf16 v[44:47], v[92:95], v[228:231], v[44:47]\n\t" \
    "ds_read_b64_tr_b16 v[246:247], v1 offset:53760\n\t" \
    "s_waitcnt lgkmcnt(4)\n\t" \
    "v_mfma_f32_16x16x32_bf16 v[40:43], v[96:99], v[232:235], v[40:43]\n\t" \
    "ds_read_b64_tr_b16 v[248:249], v1 offset:57344\n\t" \
    "v_mfma_f32_16x16x32_bf16 v[44:47], v[100:103], v[232:235], v[44:47]\n\t" \
    "ds_read_b64_tr_b16 v[250:251], v1 offset:57856\n\t" \
    "v_mfma_f32_16x16x32_bf16 v[40:43], v[104:107], v[236:239], v[40:43]\n\t" \
    "ds_read_b64_tr_b16 v[252:253], v1 offset:61440\n\t" \
    "buffer_load_dwordx4 v5, s[84:87], s49 offen offset:1024 lds\n\t" \
    "v_mfma_f32_16x16x32_bf16 v[44:47], v[108:111], v[236:239], v[44:47]\n\t" \
    "ds_read_b64_tr_b16 v[254:255], v1 offset:61952\n\t" \
      \
    "s_waitcnt vmcnt(4)\n\t" \
    "s_barrier\n\t" \
    "v_xor_b32 v4, 0x10000, v4\n\t" \
    "v_xor_b32 v20, 0x10000, v20\n\t" \
    "v_xor_b32 v21, 0x10000, v21\n\t" \
    "v_xor_b32 v22, 0x10000, v22\n\t" \
    "s_waitcnt lgkmcnt(6)\n\t" \
    "v_mfma_f32_16x16x32_bf16 v[216:219], v[240:243], v[80:83], v[216:219]\n\t" \
    "ds_read_b128 v[112:115], v4 offset:0\n\t" \
    "ds_read_b128 v[116:119], v4 offset:4096\n\t" \
    "v_and_b32 v11, 0x10000, v10\n\t" \
    "v_mfma_f32_16x16x32_bf16 v[220:223], v[240:243], v[84:87], v[220:223]\n\t" \
    "ds_read_b64_tr_b16 v[224:225], v0 offset:16384\n\t" \
    "ds_read_b64_tr_b16 v[226:227], v0 offset:16896\n\t" \
    "s_add_u32 m0, s47, 65536\n\t" \
    "s_add_u32 s49, s91, 1024\n\t" \
    "buffer_load_dwordx4 v2, s[80:83], s49 offen lds\n\t" \
    "v_or_b32 v11, v11, v9\n\t" \
    "s_waitcnt lgkmcnt(8)\n\t" \
    "v_mfma_f32_16x16x32_bf16 v[216:219], v[244:247], v[88:91], v[216:219]\n\t" \
    "ds_read_b128 v[120:123], v20 offset:0\n\t" \
    "ds_read_b128 v[124:127], v20 offset:4096\n\t" \
    "v_mfma_f32_16x16x32_bf16 v[220:223], v[244:247], v[92:95], v[220:223]\n\t" \
    "ds_read_b64_tr_b16 v[228:229], v0 offset:20480\n\t" \
    "ds_read_b64_tr_b16 v[230:231], v0 offset:20992\n\t" \
    "buffer_load_dwordx4 v3, s[80:83], s49 offen offset:1024 lds\n\t" \
    "s_waitcnt lgkmcnt(10)\n\t" \
    "v_mfma_f32_16x16x32_bf16 v[216:219], v[248:251], v[96:99], v[216:219]\n\t" \
    "ds_read_b128 v[128:131], v21 offset:0\n\t" \
    "ds_read_b128 v[132:135], v21 offset:4096\n\t" \
    "v_mfma_f32_16x16x32_bf16 v[220:223], v[248:251], v[100:103], v[220:223]\n\t" \
    "ds_read_b64_tr_b16 v[232:233], v0 offset:24576\n\t" \
    "ds_read_b64_tr_b16 v[234:235], v0 offset:25088\n\t" \
    "s_add_u32 m0, s47, 81920\n\t" \
    "s_add_u32 s49, s91, 2048\n\t" \
    "buffer_load_dwordx4 v2, s[80:83], s49 offen lds\n\t" \
    "s_waitcnt lgkmcnt(12)\n\t" \
    "v_mfma_f32_16x16x32_bf16 v[216:219], v[252:255], v[104:107], v[216:219]\n\t" \
    "ds_read_b128 v[136:139], v22 offset:0\n\t" \
    "ds_read_b128 v[140:143], v22 offset:4096\n\t" \
    "v_mfma_f32_16x16x32_bf16 v[220:223], v[252:255], v[108:111], v[220:223]\n\t" \
    "ds_read_b64_tr_b16 v[236:237], v0 offset:28672\n\t" \
    "ds_read_b64_tr_b16 v[238:239], v0 offset:29184\n\t" \
    "buffer_load_dwordx4 v3, s[80:83], s49 offen offset:1024 lds\n\t" \
      \
    "s_waitcnt vmcnt(6)\n\t" \
    "s_barrier\n\t" \
    "s_waitcnt lgkmcnt(12)\n\t" \
    "v_mfma_f32_16x16x32_bf16 v[32:35], v[224:227], v[112:115], v[32:35]\n\t" \
    "ds_read_b64_tr_b16 v[240:241], v0 offset:32768\n\t" \
    "v_mfma_f32_16x16x32_bf16 v[36:39], v[224:227], v[116:119], v[36:39]\n\t" \
    "ds_read_b64_tr_b16 v[242:243], v0 offset:33280\n\t" \
    "s_waitcnt lgkmcnt(10)\n\t" \
    "v_mfma_f32_16x16x32_bf16 v[32:35], v[228:231], v[120:123], v[32:35]\n\t" \
    "ds_read_b64_tr_b16 v[244:245], v0 offset:36864\n\t" \
    "s_add_u32 m0, s47, 98304\n\t" \
    "s_add_u32 s49, s90, 0\n\t" \
    "buffer_load_dwordx4 v2, s[80:83], s49 offen lds\n\t" \
    "v_mfma_f32_16x16x32_bf16 v[36:39], v[228:231], v[124:127], v[36:39]\n\t" \
    "ds_read_b64_tr_b16 v[246:247], v0 offset:37376\n\t" \
    "s_waitcnt lgkmcnt(8)\n\t" \
    "v_mfma_f32_16x16x32_bf16 v[32:35], v[232:235], v[128:131], v[32:35]\n\t" \
    "ds_read_b64_tr_b16 v[248:249], v0 offset:40960\n\t" \
    "v_mfma_f32_16x16x32_bf16 v[36:39], v[232:235], v[132:135], v[36:39]\n\t" \
    "ds_read_b64_tr_b16 v[250:251], v0 offset:41472\n\t" \
    "s_waitcnt lgkmcnt(6)\n\t" \
    "v_mfma_f32_16x16x32_bf16 v[32:35], v[236:239], v[136:139], v[32:35]\n\t" \
    "ds_read_b64_tr_b16 v[252:253], v0 offset:45056\n\t" \
    "buffer_load_dwordx4 v3, s[80:83], s49 offen offset:1024 lds\n\t" \
    "v_mfma_f32_16x16x32_bf16 v[36:39], v[236:239], v[140:143], v[36:39]\n\t" \
    "ds_read_b64_tr_b16 v[254:255], v0 offset:45568\n\t" \
    "s_lshr_b32 s50, s74, 1\n\t" \
    "s_cmp_lg_u32 s50, 2\n\t" \
    "s_cbranch_scc1 122f\n\t" \
    "v_mfma_f32_16x16x32_bf16 v[16:19], v[176:179], v[112:115], 0\n\t" \
    "v_mfma_f32_16x16x32_bf16 v[24:27], v[176:179], v[116:119], 0\n\t" \
    "v_mfma_f32_16x16x32_bf16 v[16:19], v[180:183], v[120:123], v[16:19]\n\t" \
    "v_mfma_f32_16x16x32_bf16 v[24:27], v[180:183], v[124:127], v[24:27]\n\t" \
    "v_mfma_f32_16x16x32_bf16 v[16:19], v[184:187], v[128:131], v[16:19]\n\t" \
    "v_mfma_f32_16x16x32_bf16 v[24:27], v[184:187], v[132:135], v[24:27]\n\t" \
    "v_mfma_f32_16x16x32_bf16 v[16:19], v[188:191], v[136:139], v[16:19]\n\t" \
    "v_mfma_f32_16x16x32_bf16 v[24:27], v[188:191], v[140:143], v[24:27]\n\t" \
    "122:\n\t" \
      \
    "s_waitcnt vmcnt(4)\n\t" \
    "s_barrier\n\t" \
    "s_waitcnt lgkmcnt(6)\n\t" \
    "v_mfma_f32_16x16x32_bf16 v[40:43], v[112:115], v[240:243], v[40:43]\n\t" \
    "ds_read_b128 v[144:147], v4 offset:49152\n\t" \
    "ds_read_b128 v[148:151], v4 offset:53248\n\t" \
    "v_mfma_f32_16x16x32_bf16 v[44:47], v[116:119], v[240:243], v[44:47]\n\t" \
    "ds_read_b64_tr_b16 v[224:225], v1 offset:0\n\t" \
    "ds_read_b64_tr_b16 v[226:227], v1 offset:512\n\t" \
    "s_add_u32 m0, s47, 114688\n\t" \
    "s_add_u32 s49, s91, 0\n\t" \
    "buffer_load_dwordx4 v2, s[80:83], s49 offen lds\n\t" \
    "s_waitcnt lgkmcnt(8)\n\t" \
    "v_mfma_f32_16x16x32_bf16 v[40:43], v[120:123], v[244:247], v[40:43]\n\t" \
    "ds_read_b128 v[152:155], v20 offset:49152\n\t" \
    "ds_read_b128 v[156:159], v20 offset:53248\n\t" \
    "v_mfma_f32_16x16x32_bf16 v[44:47], v[124:127], v[244:247], v[44:47]\n\t" \
    "ds_read_b64_tr_b16 v[228:229], v1 offset:4096\n\t" \
    "ds_read_b64_tr_b16 v[230:231], v1 offset:4608\n\t" \
    "buffer_load_dwordx4 v3, s[80:83], s49 offen offset:1024 lds\n\t" \
    "s_waitcnt lgkmcnt(10)\n\t" \
    "v_mfma_f32_16x16x32_bf16 v[40:43], v[128:131], v[248:251], v[40:43]\n\t" \
    "ds_read_b128 v[160:163], v21 offset:49152\n\t" \
    "ds_read_b128 v[164:167], v21 offset:53248\n\t" \
    "v_mfma_f32_16x16x32_bf16 v[44:47], v[132:135], v[248:251], v[44:47]\n\t" \
    "ds_read_b64_tr_b16 v[232:233], v1 offset:8192\n\t" \
    "ds_read_b64_tr_b16 v[234:235], v1 offset:8704\n\t" \
    "s_add_u32 m0, s47, 0\n\t" \
    "s_add_u32 s49, s88, 1024\n\t" \
    "buffer_load_dwordx4 v2, s[52:55], s49 offen lds\n\t" \
    "s_waitcnt lgkmcnt(12)\n\t" \
    "v_mfma_f32_16x16x32_bf16 v[40:43], v[136:139], v[252:255], v[40:43]\n\t" \
    "ds_read_b128 v[168:171], v22 offset:49152\n\t" \
    "ds_read_b128 v[172:175], v22 offset:53248\n\t" \
    "v_mfma_f32_16x16x32_bf16 v[44:47], v[140:143], v[252:255], v[44:47]\n\t" \
    "ds_read_b64_tr_b16 v[236:237], v1 offset:12288\n\t" \
    "ds_read_b64_tr_b16 v[238:239], v1 offset:12800\n\t" \
    "buffer_load_dwordx4 v3, s[52:55], s49 offen offset:1024 lds\n\t" \
      \
    "s_waitcnt vmcnt(6)\n\t" \
    "s_barrier\n\t" \
    "s_waitcnt lgkmcnt(12)\n\t" \
    "v_mfma_f32_16x16x32_bf16 v[32:35], v[224:227], v[144:147], v[32:35]\n\t" \
    "ds_read_b64_tr_b16 v[240:241], v1 offset:16384\n\t" \
    "v_mfma_f32_16x16x32_bf16 v[36:39], v[224:227], v[148:151], v[36:39]\n\t" \
    "ds_read_b64_tr_b16 v[242:243], v1 offset:16896\n\t" \
    "s_waitcnt lgkmcnt(10)\n\t" \
    "v_mfma_f32_16x16x32_bf16 v[32:35], v[228:231], v[152:155], v[32:35]\n\t" \
    "ds_read_b64_tr_b16 v[244:245], v1 offset:20480\n\t" \
    "s_add_u32 m0, s47, 16384\n\t" \
    "s_add_u32 s49, s89, 1024\n\t" \
    "buffer_load_dwordx4 v2, s[52:55], s49 offen lds\n\t" \
    "v_mfma_f32_16x16x32_bf16 v[36:39], v[228:231], v[156:159], v[36:39]\n\t" \
    "ds_read_b64_tr_b16 v[246:247], v1 offset:20992\n\t" \
    "s_waitcnt lgkmcnt(8)\n\t" \
    "v_mfma_f32_16x16x32_bf16 v[32:35], v[232:235], v[160:163], v[32:35]\n\t" \
    "ds_read_b64_tr_b16 v[248:249], v1 offset:24576\n\t" \
    "v_mfma_f32_16x16x32_bf16 v[36:39], v[232:235], v[164:167], v[36:39]\n\t" \
    "ds_read_b64_tr_b16 v[250:251], v1 offset:25088\n\t" \
    "s_waitcnt lgkmcnt(6)\n\t" \
    "v_mfma_f32_16x16x32_bf16 v[32:35], v[236:239], v[168:171], v[32:35]\n\t" \
    "ds_read_b64_tr_b16 v[252:253], v1 offset:28672\n\t" \
    "buffer_load_dwordx4 v3, s[52:55], s49 offen offset:1024 lds\n\t" \
    "v_mfma_f32_16x16x32_bf16 v[36:39], v[236:239], v[172:175], v[36:39]\n\t" \
    "ds_read_b64_tr_b16 v[254:255], v1 offset:29184\n\t" \
    "s_lshr_b32 s50, s74, 1\n\t" \
    "s_cmp_lg_u32 s50, 3\n\t" \
    "s_cbranch_scc1 132f\n\t" \
    "v_mfma_f32_16x16x32_bf16 v[16:19], v[176:179], v[144:147], 0\n\t" \
    "v_mfma_f32_16x16x32_bf16 v[24:27], v[176:179], v[148:151], 0\n\t" \
    "v_mfma_f32_16x16x32_bf16 v[16:19], v[180:183], v[152:155], v[16:19]\n\t" \
    "v_mfma_f32_16x16x32_bf16 v[24:27], v[180:183], v[156:159], v[24:27]\n\t" \
    "v_mfma_f32_16x16x32_bf16 v[16:19], v[184:187], v[160:163], v[16:19]\n\t" \
    "v_mfma_f32_16x16x32_bf16 v[24:27], v[184:187], v[164:167], v[24:27]\n\t" \
    "v_mfma_f32_16x16x32_bf16 v[16:19], v[188:191], v[168:171], v[16:19]\n\t" \
    "v_mfma_f32_16x16x32_bf16 v[24:27], v[188:191], v[172:175], v[24:27]\n\t" \
    "132:\n\t" \
      \
    "s_waitcnt vmcnt(4)\n\t" \
    "s_barrier\n\t" \
    "s_waitcnt lgkmcnt(4)\n\t" \
    "v_mfma_f32_16x16x32_bf16 v[40:43], v[144:147], v[240:243], v[40:43]\n\t" \
    "ds_read_b64_tr_b16 v[224:225], v1 offset:32768\n\t" \
    "v_mfma_f32_16x16x32_bf16 v[44:47], v[148:151], v[240:243], v[44:47]\n\t" \
    "ds_read_b64_tr_b16 v[226:227], v1 offset:33280\n\t" \
    "v_mfma_f32_16x16x32_bf16 v[40:43], v[152:155], v[244:247], v[40:43]\n\t" \
    "ds_read_b64_tr_b16 v[228:229], v1 offset:36864\n\t" \
    "s_add_u32 m0, s47, 32768\n\t" \
    "s_add_u32 s49, s90, 1024\n\t" \
    "buffer_load_dwordx4 v2, s[52:55], s49 offen lds\n\t" \
    "v_mfma_f32_16x16x32_bf16 v[44:47], v[156:159], v[244:247], v[44:47]\n\t" \
    "ds_read_b64_tr_b16 v[230:231], v1 offset:37376\n\t" \
    "v_cvt_pk_bf16_f32 v20, v32, v33\n\t" \
    "v_cvt_pk_bf16_f32 v22, v36, v37\n\t" \
    "s_waitcnt lgkmcnt(4)\n\t" \
    "v_mfma_f32_16x16x32_bf16 v[40:43], v[160:163], v[248:251], v[40:43]\n\t" \
    "ds_read_b64_tr_b16 v[232:233], v1 offset:40960\n\t" \
    "v_cvt_pk_bf16_f32 v21, v34, v35\n\t" \
    "v_cvt_pk_bf16_f32 v23, v38, v39\n\t" \
    "v_mfma_f32_16x16x32_bf16 v[44:47], v[164:167], v[248:251], v[44:47]\n\t" \
    "ds_read_b64_tr_b16 v[234:235], v1 offset:41472\n\t" \
    "s_nop 0\n\t" \
    "v_permlane16_swap_b32 v20, v22\n\t" \
    "v_mfma_f32_16x16x32_bf16 v[40:43], v[168:171], v[252:255], v[40:43]\n\t" \
    "ds_read_b64_tr_b16 v[236:237], v1 offset:45056\n\t" \
    "buffer_load_dwordx4 v3, s[52:55], s49 offen offset:1024 lds\n\t" \
    "v_permlane16_swap_b32 v21, v23\n\t" \
    "v_mfma_f32_16x16x32_bf16 v[44:47], v[172:175], v[252:255], v[44:47]\n\t" \
    "ds_read_b64_tr_b16 v[238:239], v1 offset:45568\n\t" \
    "buffer_store_dwordx4 v[20:23], v9, s[56:59], s60 offen offset:0 sc1\n\t" \
      \
    "s_waitcnt lgkmcnt(4)\n\t" \
    "v_mfma_f32_16x16x32_bf16 v[216:219], v[224:227], v[112:115], v[216:219]\n\t" \
    "ds_read_b64_tr_b16 v[240:241], v1 offset:49152\n\t" \
    "v_mfma_f32_16x16x32_bf16 v[220:223], v[224:227], v[116:119], v[220:223]\n\t" \
    "ds_read_b64_tr_b16 v[242:243], v1 offset:49664\n\t" \
    "v_mfma_f32_16x16x32_bf16 v[216:219], v[228:231], v[120:123], v[216:219]\n\t" \
    "ds_read_b64_tr_b16 v[244:245], v1 offset:53248\n\t" \
    "s_add_u32 m0, s47, 49152\n\t" \
    "s_add_u32 s49, s91, 1024\n\t" \
    "buffer_load_dwordx4 v2, s[52:55], s49 offen lds\n\t" \
    "v_mfma_f32_16x16x32_bf16 v[220:223], v[228:231], v[124:127], v[220:223]\n\t" \
    "ds_read_b64_tr_b16 v[246:247], v1 offset:53760\n\t" \
    "v_cvt_pk_bf16_f32 v20, v40, v41\n\t" \
    "v_cvt_pk_bf16_f32 v22, v44, v45\n\t" \
    "s_waitcnt lgkmcnt(4)\n\t" \
    "v_mfma_f32_16x16x32_bf16 v[216:219], v[232:235], v[128:131], v[216:219]\n\t" \
    "ds_read_b64_tr_b16 v[248:249], v1 offset:57344\n\t" \
    "v_cvt_pk_bf16_f32 v21, v42, v43\n\t" \
    "v_cvt_pk_bf16_f32 v23, v46, v47\n\t" \
    "v_mfma_f32_16x16x32_bf16 v[220:223], v[232:235], v[132:135], v[220:223]\n\t" \
    "ds_read_b64_tr_b16 v[250:251], v1 offset:57856\n\t" \
    "s_nop 0\n\t" \
    "v_permlane16_swap_b32 v20, v22\n\t" \
    "v_mfma_f32_16x16x32_bf16 v[216:219], v[236:239], v[136:139], v[216:219]\n\t" \
    "ds_read_b64_tr_b16 v[252:253], v1 offset:61440\n\t" \
    "buffer_load_dwordx4 v3, s[52:55], s49 offen offset:1024 lds\n\t" \
    "v_permlane16_swap_b32 v21, v23\n\t" \
    "v_mfma_f32_16x16x32_bf16 v[220:223], v[236:239], v[140:143], v[220:223]\n\t" \
    "ds_read_b64_tr_b16 v[254:255], v1 offset:61952\n\t" \
    "buffer_store_dwordx4 v[20:23], v9, s[56:59], s60 offen offset:1024 sc1\n\t" \
      \
    "s_waitcnt vmcnt(6)\n\t" \
    "s_barrier\n\t" \
    "s_waitcnt lgkmcnt(4)\n\t" \
    "v_mfma_f32_16x16x32_bf16 v[216:219], v[240:243], v[144:147], v[216:219]\n\t" \
    "ds_read_b64_tr_b16 v[224:225], v0 offset:0\n\t" \
    "v_mfma_f32_16x16x32_bf16 v[220:223], v[240:243], v[148:151], v[220:223]\n\t" \
    "ds_read_b64_tr_b16 v[226:227], v0 offset:512\n\t" \
    "v_mfma_f32_16x16x32_bf16 v[216:219], v[244:247], v[152:155], v[216:219]\n\t" \
    "ds_read_b64_tr_b16 v[228:229], v0 offset:4096\n\t" \
    "s_add_u32 m0, s47, 65536\n\t" \
    "s_add_u32 s49, s88, 2048\n\t" \
    "buffer_load_dwordx4 v2, s[52:55], s49 offen lds\n\t" \
    "v_mfma_f32_16x16x32_bf16 v[220:223], v[244:247], v[156:159], v[220:223]\n\t" \
    "ds_read_b64_tr_b16 v[230:231], v0 offset:4608\n\t" \
    "s_waitcnt lgkmcnt(4)\n\t" \
    "v_mfma_f32_16x16x32_bf16 v[216:219], v[248:251], v[160:163], v[216:219]\n\t" \
    "ds_read_b64_tr_b16 v[232:233], v0 offset:8192\n\t" \
    "v_mfma_f32_16x16x32_bf16 v[220:223], v[248:251], v[164:167], v[220:223]\n\t" \
    "ds_read_b64_tr_b16 v[234:235], v0 offset:8704\n\t" \
    "v_mfma_f32_16x16x32_bf16 v[216:219], v[252:255], v[168:171], v[216:219]\n\t" \
    "ds_read_b64_tr_b16 v[236:237], v0 offset:12288\n\t" \
    "buffer_load_dwordx4 v3, s[52:55], s49 offen offset:1024 lds\n\t" \
    "v_mfma_f32_16x16x32_bf16 v[220:223], v[252:255], v[172:175], v[220:223]\n\t" \
    "ds_read_b64_tr_b16 v[238:239], v0 offset:12800\n\t" \
      \
    "s_waitcnt lgkmcnt(4)\n\t" \
    "v_mfma_f32_16x16x32_bf16 v[32:35], v[224:227], v[48:51], 0\n\t" \
    "ds_read_b64_tr_b16 v[240:241], v0 offset:16384\n\t" \
    "v_mfma_f32_16x16x32_bf16 v[36:39], v[224:227], v[52:55], 0\n\t" \
    "ds_read_b64_tr_b16 v[242:243], v0 offset:16896\n\t" \
    "v_mfma_f32_16x16x32_bf16 v[32:35], v[228:231], v[56:59], v[32:35]\n\t" \
    "ds_read_b64_tr_b16 v[244:245], v0 offset:20480\n\t" \
    "s_add_u32 m0, s47, 81920\n\t" \
    "s_add_u32 s49, s89, 2048\n\t" \
    "buffer_load_dwordx4 v2, s[52:55], s49 offen lds\n\t" \
    "v_mfma_f32_16x16x32_bf16 v[36:39], v[228:231], v[60:63], v[36:39]\n\t" \
    "ds_read_b64_tr_b16 v[246:247], v0 offset:20992\n\t" \
    "s_mov_b32 s50, 0x3eb8aa3b\n\t" \
    "v_pk_mul_f32 v[216:217], v[216:217], s[50:51] op_sel_hi:[1,0]\n\t" \
    "v_pk_mul_f32 v[218:219], v[218:219], s[50:51] op_sel_hi:[1,0]\n\t" \
    "s_waitcnt lgkmcnt(4)\n\t" \
    "v_mfma_f32_16x16x32_bf16 v[32:35], v[232:235], v[64:67], v[32:35]\n\t" \
    "ds_read_b64_tr_b16 v[248:249], v0 offset:24576\n\t" \
    "v_pk_mul_f32 v[220:221], v[220:221], s[50:51] op_sel_hi:[1,0]\n\t" \
    "v_pk_mul_f32 v[222:223], v[222:223], s[50:51] op_sel_hi:[1,0]\n\t" \
    "v_cvt_pk_bf16_f32 v28, v216, v217\n\t" \
    "v_mfma_f32_16x16x32_bf16 v[36:39], v[232:235], v[68:71], v[36:39]\n\t" \
    "ds_read_b64_tr_b16 v[250:251], v0 offset:25088\n\t" \
    "v_cvt_pk_bf16_f32 v30, v220, v221\n\t" \
    "v_cvt_pk_bf16_f32 v29, v218, v219\n\t" \
    "v_cvt_pk_bf16_f32 v31, v222, v223\n\t" \
    "v_mfma_f32_16x16x32_bf16 v[32:35], v[236:239], v[72:75], v[32:35]\n\t" \
    "ds_read_b64_tr_b16 v[252:253], v0 offset:28672\n\t" \
    "buffer_load_dwordx4 v3, s[52:55], s49 offen offset:1024 lds\n\t" \
    "s_nop 0\n\t" \
    "v_permlane16_swap_b32 v28, v30\n\t" \
    "v_permlane16_swap_b32 v29, v31\n\t" \
    "v_mfma_f32_16x16x32_bf16 v[36:39], v[236:239], v[76:79], v[36:39]\n\t" \
    "ds_read_b64_tr_b16 v[254:255], v0 offset:29184\n\t" \
      \
    "s_waitcnt vmcnt(4)\n\t" \
    "s_barrier\n\t" \
      \
    "s_cmp_lg_u32 s74, 7\n\t" \
    "s_cbranch_scc1 50f\n\t" \
    "s_mov_b64 exec, 1\n\t" \
    "v_mov_b64 v[18:19], s[36:37]\n\t" \
    "global_store_dwordx2 v9, v[18:19], s[64:65] sc1\n\t" \
    "s_mov_b64 exec, -1\n\t" \
    "50:\n\t" \
    "s_waitcnt lgkmcnt(4)\n\t" \
    "v_mfma_f32_16x16x32_bf16 v[32:35], v[240:243], v[80:83], v[32:35]\n\t" \
    "ds_read_b64_tr_b16 v[224:225], v0 offset:32768\n\t" \
    "v_mov_b64 v[216:217], v[192:193]\n\t" \
    "v_mfma_f32_16x16x32_bf16 v[36:39], v[240:243], v[84:87], v[36:39]\n\t" \
    "ds_read_b64_tr_b16 v[226:227], v0 offset:33280\n\t" \
    "v_mov_b64 v[218:219], v[192:193]\n\t" \
    "v_mfma_f32_16x16x32_bf16 v[32:35], v[244:247], v[88:91], v[32:35]\n\t" \
    "ds_read_b64_tr_b16 v[228:229], v0 offset:36864\n\t" \
    "s_add_u32 m0, s47, 98304\n\t" \
    "s_add_u32 s49, s90, 2048\n\t" \
    "buffer_load_dwordx4 v2, s[52:55], s49 offen lds\n\t" \
    "v_mov_b64 v[220:221], v[192:193]\n\t" \
    "v_mfma_f32_16x16x32_bf16 v[36:39], v[244:247], v[92:95], v[36:39]\n\t" \
    "ds_read_b64_tr_b16 v[230:231], v0 offset:37376\n\t" \
    "v_mov_b64 v[222:223], v[192:193]\n\t" \
    "s_waitcnt lgkmcnt(4)\n\t" \
    "v_mfma_f32_16x16x32_bf16 v[32:35], v[248:251], v[96:99], v[32:35]\n\t" \
    "ds_read_b64_tr_b16 v[232:233], v0 offset:40960\n\t" \
    "v_mfma_f32_16x16x32_bf16 v[36:39], v[248:251], v[100:103], v[36:39]\n\t" \
    "ds_read_b64_tr_b16 v[234:235], v0 offset:41472\n\t" \
    "v_mfma_f32_16x16x32_bf16 v[32:35], v[252:255], v[104:107], v[32:35]\n\t" \
    "ds_read_b64_tr_b16 v[236:237], v0 offset:45056\n\t" \
    "buffer_load_dwordx4 v3, s[52:55], s49 offen offset:1024 lds\n\t" \
    "v_mfma_f32_16x16x32_bf16 v[36:39], v[252:255], v[108:111], v[36:39]\n\t" \
    "ds_read_b64_tr_b16 v[238:239], v0 offset:45568\n\t" \
      \
    "s_waitcnt lgkmcnt(4)\n\t" \
    "v_mfma_f32_16x16x32_bf16 v[32:35], v[224:227], v[112:115], v[32:35]\n\t" \
    "ds_read_b64_tr_b16 v[240:241], v0 offset:49152\n\t" \
    "v_mfma_f32_16x16x32_bf16 v[36:39], v[224:227], v[116:119], v[36:39]\n\t" \
    "ds_read_b64_tr_b16 v[242:243], v0 offset:49664\n\t" \
    "v_mfma_f32_16x16x32_bf16 v[32:35], v[228:231], v[120:123], v[32:35]\n\t" \
    "ds_read_b64_tr_b16 v[244:245], v0 offset:53248\n\t" \
    "s_add_u32 m0, s47, 114688\n\t" \
    "s_add_u32 s49, s91, 2048\n\t" \
    "buffer_load_dwordx4 v2, s[52:55], s49 offen lds\n\t" \
    "v_mfma_f32_16x16x32_bf16 v[36:39], v[228:231], v[124:127], v[36:39]\n\t" \
    "ds_read_b64_tr_b16 v[246:247], v0 offset:53760\n\t" \
    "s_waitcnt lgkmcnt(4)\n\t" \
    "v_mfma_f32_16x16x32_bf16 v[32:35], v[232:235], v[128:131], v[32:35]\n\t" \
    "ds_read_b64_tr_b16 v[248:249], v0 offset:57344\n\t" \
    "v_mfma_f32_16x16x32_bf16 v[36:39], v[232:235], v[132:135], v[36:39]\n\t" \
    "ds_read_b64_tr_b16 v[250:251], v0 offset:57856\n\t" \
    "v_mfma_f32_16x16x32_bf16 v[32:35], v[236:239], v[136:139], v[32:35]\n\t" \
    "ds_read_b64_tr_b16 v[252:253], v0 offset:61440\n\t" \
    "buffer_load_dwordx4 v3, s[52:55], s49 offen offset:1024 lds\n\t" \
    "v_mfma_f32_16x16x32_bf16 v[36:39], v[236:239], v[140:143], v[36:39]\n\t" \
    "ds_read_b64_tr_b16 v[254:255], v0 offset:61952\n\t" \
      \
    "s_waitcnt vmcnt(4)\n\t" \
    "s_barrier\n\t" \
    "s_waitcnt lgkmcnt(4)\n\t" \
    "v_mfma_f32_16x16x32_bf16 v[32:35], v[240:243], v[144:147], v[32:35]\n\t" \
    "ds_read_b64_tr_b16 v[224:225], v1 offset:0\n\t" \
    "v_mfma_f32_16x16x32_bf16 v[36:39], v[240:243], v[148:151], v[36:39]\n\t" \
    "ds_read_b64_tr_b16 v[226:227], v1 offset:512\n\t" \
    "v_mfma_f32_16x16x32_bf16 v[32:35], v[244:247], v[152:155], v[32:35]\n\t" \
    "ds_read_b64_tr_b16 v[228:229], v1 offset:4096\n\t" \
    "s_add_u32 m0, s47, 0\n\t" \
    "s_add_u32 s49, s88, 0\n\t" \
    "buffer_load_dwordx4 v2, s[52:55], s49 offen lds\n\t" \
    "v_mfma_f32_16x16x32_bf16 v[36:39], v[244:247], v[156:159], v[36:39]\n\t" \
    "ds_read_b64_tr_b16 v[230:231], v1 offset:4608\n\t" \
    "s_waitcnt lgkmcnt(4)\n\t" \
    "v_mfma_f32_16x16x32_bf16 v[32:35], v[248:251], v[160:163], v[32:35]\n\t" \
    "ds_read_b64_tr_b16 v[232:233], v1 offset:8192\n\t" \
    "v_mfma_f32_16x16x32_bf16 v[36:39], v[248:251], v[164:167], v[36:39]\n\t" \
    "ds_read_b64_tr_b16 v[234:235], v1 offset:8704\n\t" \
    "v_mfma_f32_16x16x32_bf16 v[32:35], v[252:255], v[168:171], v[32:35]\n\t" \
    "ds_read_b64_tr_b16 v[236:237], v1 offset:12288\n\t" \
    "buffer_load_dwordx4 v3, s[52:55], s49 offen offset:1024 lds\n\t" \
    "v_mfma_f32_16x16x32_bf16 v[36:39], v[252:255], v[172:175], v[36:39]\n\t" \
    "ds_read_b64_tr_b16 v[238:239], v1 offset:12800\n\t" \
      \
    "s_waitcnt lgkmcnt(4)\n\t" \
    "v_mfma_f32_16x16x32_bf16 v[40:43], v[48:51], v[224:227], 0\n\t" \
    "ds_read_b64_tr_b16 v[240:241], v1 offset:16384\n\t" \
    "v_mfma_f32_16x16x32_bf16 v[44:47], v[52:55], v[224:227], 0\n\t" \
    "ds_read_b64_tr_b16 v[242:243], v1 offset:16896\n\t" \
    "v_mfma_f32_16x16x32_bf16 v[40:43], v[56:59], v[228:231], v[40:43]\n\t" \
    "ds_read_b64_tr_b16 v[244:245], v1 offset:20480\n\t" \
    "s_add_u32 m0, s47, 16384\n\t" \
    "s_add_u32 s49, s89, 0\n\t" \
    "buffer_load_dwordx4 v2, s[52:55], s49 offen lds\n\t" \
    "v_mfma_f32_16x16x32_bf16 v[44:47], v[60:63], v[228:231], v[44:47]\n\t" \
    "ds_read_b64_tr_b16 v[246:247], v1 offset:20992\n\t" \
    "v_cvt_pk_bf16_f32 v184, v32, v33\n\t" \
    "v_cvt_pk_bf16_f32 v186, v36, v37\n\t" \
    "s_waitcnt lgkmcnt(4)\n\t" \
    "v_mfma_f32_16x16x32_bf16 v[40:43], v[64:67], v[232:235], v[40:43]\n\t" \
    "ds_read_b64_tr_b16 v[248:249], v1 offset:24576\n\t" \
    "v_cvt_pk_bf16_f32 v185, v34, v35\n\t" \
    "v_cvt_pk_bf16_f32 v187, v38, v39\n\t" \
    "v_mfma_f32_16x16x32_bf16 v[44:47], v[68:71], v[232:235], v[44:47]\n\t" \
    "ds_read_b64_tr_b16 v[250:251], v1 offset:25088\n\t" \
    "s_nop 0\n\t" \
    "v_permlane16_swap_b32 v184, v186\n\t" \
    "v_mfma_f32_16x16x32_bf16 v[40:43], v[72:75], v[236:239], v[40:43]\n\t" \
    "ds_read_b64_tr_b16 v[252:253], v1 offset:28672\n\t" \
    "buffer_load_dwordx4 v3, s[52:55], s49 offen offset:1024 lds\n\t" \
    "v_permlane16_swap_b32 v185, v187\n\t" \
    "v_mfma_f32_16x16x32_bf16 v[44:47], v[76:79], v[236:239], v[44:47]\n\t" \
    "ds_read_b64_tr_b16 v[254:255], v1 offset:29184\n\t" \
      \
    "s_waitcnt vmcnt(4)\n\t" \
    "s_barrier\n\t" \
    "s_waitcnt lgkmcnt(4)\n\t" \
    "v_mfma_f32_16x16x32_bf16 v[40:43], v[80:83], v[240:243], v[40:43]\n\t" \
    "ds_read_b64_tr_b16 v[224:225], v1 offset:32768\n\t" \
    "v_mfma_f32_16x16x32_bf16 v[44:47], v[84:87], v[240:243], v[44:47]\n\t" \
    "ds_read_b64_tr_b16 v[226:227], v1 offset:33280\n\t" \
    "v_mfma_f32_16x16x32_bf16 v[40:43], v[88:91], v[244:247], v[40:43]\n\t" \
    "ds_read_b64_tr_b16 v[228:229], v1 offset:36864\n\t" \
    "s_add_u32 m0, s47, 32768\n\t" \
    "s_add_u32 s49, s90, 0\n\t" \
    "buffer_load_dwordx4 v2, s[52:55], s49 offen lds\n\t" \
    "v_mfma_f32_16x16x32_bf16 v[44:47], v[92:95], v[244:247], v[44:47]\n\t" \
    "ds_read_b64_tr_b16 v[230:231], v1 offset:37376\n\t" \
    "s_waitcnt lgkmcnt(4)\n\t" \
    "v_mfma_f32_16x16x32_bf16 v[40:43], v[96:99], v[248:251], v[40:43]\n\t" \
    "ds_read_b64_tr_b16 v[232:233], v1 offset:40960\n\t" \
    "v_mfma_f32_16x16x32_bf16 v[44:47], v[100:103], v[248:251], v[44:47]\n\t" \
    "ds_read_b64_tr_b16 v[234:235], v1 offset:41472\n\t" \
    "v_mfma_f32_16x16x32_bf16 v[40:43], v[104:107], v[252:255], v[40:43]\n\t" \
    "ds_read_b64_tr_b16 v[236:237], v1 offset:45056\n\t" \
    "buffer_load_dwordx4 v3, s[52:55], s49 offen offset:1024 lds\n\t" \
    "v_mfma_f32_16x16x32_bf16 v[44:47], v[108:111], v[252:255], v[44:47]\n\t" \
    "ds_read_b64_tr_b16 v[238:239], v1 offset:45568\n\t" \
      \
    "s_waitcnt lgkmcnt(4)\n\t" \
    "v_mfma_f32_16x16x32_bf16 v[40:43], v[112:115], v[224:227], v[40:43]\n\t" \
    "ds_read_b64_tr_b16 v[240:241], v1 offset:49152\n\t" \
    "v_mfma_f32_16x16x32_bf16 v[44:47], v[116:119], v[224:227], v[44:47]\n\t" \
    "ds_read_b64_tr_b16 v[242:243], v1 offset:49664\n\t" \
    "v_mfma_f32_16x16x32_bf16 v[40:43], v[120:123], v[228:231], v[40:43]\n\t" \
    "ds_read_b64_tr_b16 v[244:245], v1 offset:53248\n\t" \
    "s_add_u32 m0, s47, 49152\n\t" \
    "s_add_u32 s49, s91, 0\n\t" \
    "buffer_load_dwordx4 v2, s[52:55], s49 offen lds\n\t" \
    "v_mfma_f32_16x16x32_bf16 v[44:47], v[124:127], v[228:231], v[44:47]\n\t" \
    "ds_read_b64_tr_b16 v[246:247], v1 offset:53760\n\t" \
    "s_waitcnt lgkmcnt(4)\n\t" \
    "v_mfma_f32_16x16x32_bf16 v[40:43], v[128:131], v[232:235], v[40:43]\n\t" \
    "ds_read_b64_tr_b16 v[248:249], v1 offset:57344\n\t" \
    "v_mfma_f32_16x16x32_bf16 v[44:47], v[132:135], v[232:235], v[44:47]\n\t" \
    "ds_read_b64_tr_b16 v[250:251], v1 offset:57856\n\t" \
    "v_mfma_f32_16x16x32_bf16 v[40:43], v[136:139], v[236:239], v[40:43]\n\t" \
    "ds_read_b64_tr_b16 v[252:253], v1 offset:61440\n\t" \
    "buffer_load_dwordx4 v3, s[52:55], s49 offen offset:1024 lds\n\t" \
    "v_mfma_f32_16x16x32_bf16 v[44:47], v[140:143], v[236:239], v[44:47]\n\t" \
    "ds_read_b64_tr_b16 v[254:255], v1 offset:61952\n\t" \
    "global_load_dwordx2 v[188:189], v14, s[66:67] sc1\n\t" \
      \
    "s_waitcnt vmcnt(5)\n\t" \
    "s_barrier\n\t" \
    "s_waitcnt lgkmcnt(4)\n\t" \
    "v_mfma_f32_16x16x32_bf16 v[40:43], v[144:147], v[240:243], v[40:43]\n\t" \
    "ds_read_b64_tr_b16 v[224:225], v0 offset:0\n\t" \
    "v_mov_b64 v[18:19], 0\n\t" \
    "v_mfma_f32_16x16x32_bf16 v[44:47], v[148:151], v[240:243], v[44:47]\n\t" \
    "ds_read_b64_tr_b16 v[226:227], v0 offset:512\n\t" \
    "v_mfma_f32_16x16x32_bf16 v[40:43], v[152:155], v[244:247], v[40:43]\n\t" \
    "ds_read_b64_tr_b16 v[228:229], v0 offset:4096\n\t" \
    "v_mfma_f32_16x16x32_bf16 v[44:47], v[156:159], v[244:247], v[44:47]\n\t" \
    "ds_read_b64_tr_b16 v[230:231], v0 offset:4608\n\t" \
    "s_waitcnt lgkmcnt(4)\n\t" \
    "v_mfma_f32_16x16x32_bf16 v[40:43], v[160:163], v[248:251], v[40:43]\n\t" \
    "ds_read_b64_tr_b16 v[232:233], v0 offset:8192\n\t" \
    "v_mfma_f32_16x16x32_bf16 v[44:47], v[164:167], v[248:251], v[44:47]\n\t" \
    "ds_read_b64_tr_b16 v[234:235], v0 offset:8704\n\t" \
    "v_mfma_f32_16x16x32_bf16 v[40:43], v[168:171], v[252:255], v[40:43]\n\t" \
    "ds_read_b64_tr_b16 v[236:237], v0 offset:12288\n\t" \
    "v_mfma_f32_16x16x32_bf16 v[44:47], v[172:175], v[252:255], v[44:47]\n\t" \
    "ds_read_b64_tr_b16 v[238:239], v0 offset:12800\n\t" \
      \
    "s_waitcnt vmcnt(0)\n\t" \
      \
    "v_cmp_ne_u64_e64 s[42:43], s[36:37], v[188:189]\n\t" \
    "s_cmp_lg_u64 s[42:43], 0\n\t" \
    "s_cbranch_scc1 200f\n\t" \
    "202:\n\t" \
    "s_waitcnt lgkmcnt(4)\n\t" \
    "v_mfma_f32_16x16x32_bf16 v[32:35], v[224:227], v[48:51], 0\n\t" \
    "ds_read_b64_tr_b16 v[240:241], v0 offset:16384\n\t" \
    "v_mfma_f32_16x16x32_bf16 v[36:39], v[224:227], v[52:55], 0\n\t" \
    "ds_read_b64_tr_b16 v[242:243], v0 offset:16896\n\t" \
    "v_mfma_f32_16x16x32_bf16 v[32:35], v[228:231], v[56:59], v[32:35]\n\t" \
    "ds_read_b64_tr_b16 v[244:245], v0 offset:20480\n\t" \
    "global_load_dwordx4 v[176:179], v11, s[40:41] offset:0 sc1\n\t" \
    "v_mfma_f32_16x16x32_bf16 v[36:39], v[228:231], v[60:63], v[36:39]\n\t" \
    "ds_read_b64_tr_b16 v[246:247], v0 offset:20992\n\t" \
    "v_cvt_pk_bf16_f32 v20, v40, v41\n\t" \
    "v_cvt_pk_bf16_f32 v22, v44, v45\n\t" \
    "s_waitcnt lgkmcnt(4)\n\t" \
    "v_mfma_f32_16x16x32_bf16 v[32:35], v[232:235], v[64:67], v[32:35]\n\t" \
    "ds_read_b64_tr_b16 v[248:249], v0 offset:24576\n\t" \
    "v_cvt_pk_bf16_f32 v21, v42, v43\n\t" \
    "v_cvt_pk_bf16_f32 v23, v46, v47\n\t" \
    "v_mfma_f32_16x16x32_bf16 v[36:39], v[232:235], v[68:71], v[36:39]\n\t" \
    "ds_read_b64_tr_b16 v[250:251], v0 offset:25088\n\t" \
    "s_nop 0\n\t" \
    "v_permlane16_swap_b32 v20, v22\n\t" \
    "v_mfma_f32_16x16x32_bf16 v[32:35], v[236:239], v[72:75], v[32:35]\n\t" \
    "ds_read_b64_tr_b16 v[252:253], v0 offset:28672\n\t" \
    "s_mov_b64 exec, s[44:45]\n\t" \
    "global_load_dwordx4 v[192:195], v10, s[40:41] offset:1024 sc1\n\t" \
    "global_load_dwordx4 v[196:199], v10, s[40:41] offset:1280 sc1\n\t" \
    "s_mov_b64 exec, -1\n\t" \
    "v_permlane16_swap_b32 v21, v23\n\t" \
    "v_mfma_f32_16x16x32_bf16 v[36:39], v[236:239], v[76:79], v[36:39]\n\t" \
    "ds_read_b64_tr_b16 v[254:255], v0 offset:29184\n\t" \
      \
    "s_cmp_lg_u32 s73, 1\n\t" \
    "s_cbranch_scc1 58f\n\t" \
    "buffer_store_dwordx4 v[184:187], v9, s[56:59], s61 offen offset:2048 sc1\n\t" \
    "buffer_store_dwordx4 v[20:23], v9, s[56:59], s61 offen offset:3072 sc1\n\t" \
    "58:\n\t" \
    "buffer_store_dwordx4 v[184:187], v9, s[56:59], s61 offen offset:0 sc1\n\t" \
    "buffer_store_dwordx4 v[20:23], v9, s[56:59], s61 offen offset:1024 sc1\n\t" \
      \
    "s_waitcnt vmcnt(6)\n\t" \
    "s_barrier\n\t" \
    "ds_write_b128 v7, v[28:31]\n\t" \
    "ds_write_b32 v6, v16\n\t" \
    "ds_write_b32 v6, v24 offset:64\n\t" \
    "v_xor_b32 v7, 0x400, v7\n\t" \
    "s_waitcnt lgkmcnt(7)\n\t" \
    "v_mfma_f32_16x16x32_bf16 v[32:35], v[240:243], v[80:83], v[32:35]\n\t" \
    "ds_read_b64_tr_b16 v[224:225], v0 offset:32768\n\t" \
    "v_mfma_f32_16x16x32_bf16 v[36:39], v[240:243], v[84:87], v[36:39]\n\t" \
    "ds_read_b64_tr_b16 v[226:227], v0 offset:33280\n\t" \
    "v_mfma_f32_16x16x32_bf16 v[32:35], v[244:247], v[88:91], v[32:35]\n\t" \
    "ds_read_b64_tr_b16 v[228:229], v0 offset:36864\n\t" \
    "global_load_dwordx4 v[180:183], v11, s[40:41] offset:2048 sc1\n\t" \
    "v_mfma_f32_16x16x32_bf16 v[36:39], v[244:247], v[92:95], v[36:39]\n\t" \
    "ds_read_b64_tr_b16 v[230:231], v0 offset:37376\n\t" \
    "s_waitcnt lgkmcnt(4)\n\t" \
    "s_barrier\n\t" \
    "s_setprio 1\n\t" \
    "v_mfma_f32_16x16x32_bf16 v[32:35], v[248:251], v[96:99], v[32:35]\n\t" \
    "ds_read_b64_tr_b16 v[232:233], v0 offset:40960\n\t" \
    "v_mfma_f32_16x16x32_bf16 v[36:39], v[248:251], v[100:103], v[36:39]\n\t" \
    "ds_read_b64_tr_b16 v[234:235], v0 offset:41472\n\t" \
    "v_mfma_f32_16x16x32_bf16 v[32:35], v[252:255], v[104:107], v[32:35]\n\t" \
    "ds_read_b64_tr_b16 v[236:237], v0 offset:45056\n\t" \
    "s_mov_b64 exec, s[44:45]\n\t" \
    "global_load_dwordx4 v[200:203], v10, s[40:41] offset:3072 sc1\n\t" \
    "global_load_dwordx4 v[204:207], v10, s[40:41] offset:3328 sc1\n\t" \
    "s_mov_b64 exec, -1\n\t" \
    "v_mfma_f32_16x16x32_bf16 v[36:39], v[252:255], v[108:111], v[36:39]\n\t" \
    "ds_read_b64_tr_b16 v[238:239], v0 offset:45568\n\t" \
    "v_mfma_f32_32x32x8bf16_1k v[48:63], v[18:19], v[18:19], 0\n\t" \
      \
    "s_waitcnt lgkmcnt(4)\n\t" \
    "v_mfma_f32_16x16x32_bf16 v[32:35], v[224:227], v[112:115], v[32:35]\n\t" \
    "ds_read_b64_tr_b16 v[240:241], v0 offset:49152\n\t" \
    "v_mfma_f32_16x16x32_bf16 v[36:39], v[224:227], v[116:119], v[36:39]\n\t" \
    "ds_read_b64_tr_b16 v[242:243], v0 offset:49664\n\t" \
    "v_mfma_f32_16x16x32_bf16 v[32:35], v[228:231], v[120:123], v[32:35]\n\t" \
    "ds_read_b64_tr_b16 v[244:245], v0 offset:53248\n\t" \
    "s_addk_i32 s40, 0x1000\n\t" \
    "s_bitset0_b32 s40, s48\n\t" \
    "global_load_dwordx4 v[184:187], v11, s[40:41] offset:0 sc1\n\t" \
    "v_mfma_f32_16x16x32_bf16 v[36:39], v[228:231], v[124:127], v[36:39]\n\t" \
    "ds_read_b64_tr_b16 v[246:247], v0 offset:53760\n\t" \
    "s_waitcnt lgkmcnt(4)\n\t" \
    "v_mfma_f32_16x16x32_bf16 v[32:35], v[232:235], v[128:131], v[32:35]\n\t" \
    "ds_read_b64_tr_b16 v[248:249], v0 offset:57344\n\t" \
    "v_mfma_f32_16x16x32_bf16 v[36:39], v[232:235], v[132:135], v[36:39]\n\t" \
    "ds_read_b64_tr_b16 v[250:251], v0 offset:57856\n\t" \
    "v_mfma_f32_16x16x32_bf16 v[32:35], v[236:239], v[136:139], v[32:35]\n\t" \
    "ds_read_b64_tr_b16 v[252:253], v0 offset:61440\n\t" \
    "s_mov_b64 exec, s[44:45]\n\t" \
    "global_load_dwordx4 v[208:211], v10, s[40:41] offset:1024 sc1\n\t" \
    "global_load_dwordx4 v[212:215], v10, s[40:41] offset:1280 sc1\n\t" \
    "s_mov_b64 exec, -1\n\t" \
    "v_mfma_f32_16x16x32_bf16 v[36:39], v[236:239], v[140:143], v[36:39]\n\t" \
    "ds_read_b64_tr_b16 v[254:255], v0 offset:61952\n\t" \
    "v_mfma_f32_32x32x8bf16_1k v[64:79], v[18:19], v[18:19], 0\n\t" \
      \
    "s_waitcnt lgkmcnt(4)\n\t" \
    "v_mfma_f32_16x16x32_bf16 v[32:35], v[240:243], v[144:147], v[32:35]\n\t" \
    "ds_read_b128 v[24:27], v7\n\t" \
    "v_mfma_f32_16x16x32_bf16 v[36:39], v[240:243], v[148:151], v[36:39]\n\t" \
    "v_mfma_f32_16x16x32_bf16 v[32:35], v[244:247], v[152:155], v[32:35]\n\t" \
    "v_mfma_f32_16x16x32_bf16 v[36:39], v[244:247], v[156:159], v[36:39]\n\t" \
    "s_waitcnt lgkmcnt(1)\n\t" \
    "v_mfma_f32_16x16x32_bf16 v[32:35], v[248:251], v[160:163], v[32:35]\n\t" \
    "v_mfma_f32_16x16x32_bf16 v[36:39], v[248:251], v[164:167], v[36:39]\n\t" \
    "v_mfma_f32_16x16x32_bf16 v[32:35], v[252:255], v[168:171], v[32:35]\n\t" \
    "v_mfma_f32_16x16x32_bf16 v[36:39], v[252:255], v[172:175], v[36:39]\n\t" \
    "s_waitcnt vmcnt(8) lgkmcnt(0)\n\t" \
    "v_mfma_f32_32x32x16_bf16 v[224:239], v[176:179], v[28:31], 0\n\t" \
    "s_mov_b32 s54, -1\n\t" \
    "s_abs_i32 s46, s75\n\t" \
    "s_ashr_i32 s52, s75, 31\n\t" \
    "s_lshr_b32 s53, s46, 3\n\t" \
    "s_or_b32 s52, s52, 1\n\t" \
    "s_add_u32 m0, s53, 65536\n\t" \
    "s_lshl_b32 s53, s52, 10\n\t" \
    "s_lshl_b32 s52, s52, 13\n\t" \
    "s_mov_b32 s50, 0x3eb8aa3b\n\t" \
    "v_pk_mul_f32 v[32:33], v[32:33], s[50:51] op_sel_hi:[1,0]\n\t" \
    "v_pk_mul_f32 v[34:35], v[34:35], s[50:51] op_sel_hi:[1,0]\n\t" \
    "v_pk_mul_f32 v[36:37], v[36:37], s[50:51] op_sel_hi:[1,0]\n\t" \
    "v_pk_mul_f32 v[38:39], v[38:39], s[50:51] op_sel_hi:[1,0]\n\t" \
    "v_cvt_pk_bf16_f32 v20, v32, v33\n\t" \
    "v_cvt_pk_bf16_f32 v22, v36, v37\n\t" \
    "v_cvt_pk_bf16_f32 v21, v34, v35\n\t" \
    "v_cvt_pk_bf16_f32 v23, v38, v39\n\t" \
    "s_nop 0\n\t" \
    "v_permlane16_swap_b32 v20, v22\n\t" \
    "v_permlane16_swap_b32 v21, v23\n\t" \
    "s_mov_b64 vcc, -1\n\t" \
    "s_cmp_ge_u32 s74, 4\n\t" \
    "s_cbranch_scc1 52f\n\t" \
    "s_setprio 0\n\t" \
    "53:\n\t" \
      \
    "s_mov_b32 s47, 4\n\t" \
    "v_exp_f32 v224, v224\n\t" \
    "v_exp_f32 v225, v225\n\t" \
    "s_branch 2f\n\t" \
    ".p2align 6\n\t" \
    "1:\n\t" \
    "v_exp_f32 v224, v224\n\t" \
    "v_exp_f32 v225, v225\n\t" \
    "v_smfmac_f32_32x32x32_bf16 v[64:79], v[220:223], v[248:255], v8\n\t" \
    "2:\n\t" \
    "s_mov_b64 exec, s[44:45]\n\t" \
    "global_load_dwordx4 v[216:219], v10, s[40:41] offset:3072 sc1\n\t" \
    "global_load_dwordx4 v[220:223], v10, s[40:41] offset:3328 sc1\n\t" \
    "s_mov_b64 exec, -1\n\t" \
    "v_exp_f32 v226, v226\n\t" \
    "v_exp_f32 v227, v227\n\t" \
    "v_exp_f32 v228, v228\n\t" \
    "v_mfma_f32_32x32x16_bf16 v[240:255], v[176:179], v[24:27], 0\n\t" \
    "global_load_dwordx4 v[188:191], v11, s[40:41] offset:2048 sc1\n\t" \
    "v_exp_f32 v229, v229\n\t" \
    "v_exp_f32 v230, v230\n\t" \
    "v_exp_f32 v231, v231\n\t" \
    "v_exp_f32 v232, v232\n\t" \
    "v_exp_f32 v233, v233\n\t" \
    "v_exp_f32 v234, v234\n\t" \
    "v_exp_f32 v235, v235\n\t" \
    "v_smfmac_f32_32x32x32_bf16 v[48:63], v[192:195], v[224:231], v8\n\t" \
    "buffer_load_dwordx4 v12, s[76:79], s46 offen lds\n\t" \
    "s_cmp_le_u32 s47, 2\n\t" \
    "s_cbranch_scc1 3f\n\t" \
    "4:\n\t" \
    "v_exp_f32 v236, v236\n\t" \
    "v_exp_f32 v237, v237\n\t" \
    "v_exp_f32 v238, v238\n\t" \
    "v_exp_f32 v239, v239\n\t" \
    "v_exp_f32 v240, v240\n\t" \
    "v_exp_f32 v241, v241\n\t" \
    "v_smfmac_f32_32x32x32_bf16 v[48:63], v[196:199], v[232:239], v8\n\t" \
    "s_addk_i32 s40, 0x1000\n\t" \
    "s_bitset0_b32 s40, s48\n\t" \
    "v_exp_f32 v242, v242\n\t" \
    "v_exp_f32 v243, v243\n\t" \
    "v_exp_f32 v244, v244\n\t" \
    "s_waitcnt vmcnt(7)\n\t" \
    "v_mfma_f32_32x32x16_bf16 v[224:239], v[180:183], v[28:31], 0\n\t" \
    "v_exp_f32 v245, v245\n\t" \
    "v_exp_f32 v246, v246\n\t" \
    "v_exp_f32 v247, v247\n\t" \
    "v_exp_f32 v248, v248\n\t" \
    "v_exp_f32 v249, v249\n\t" \
    "v_exp_f32 v250, v250\n\t" \
    "v_exp_f32 v251, v251\n\t" \
    "v_smfmac_f32_32x32x32_bf16 v[64:79], v[192:195], v[240:247], v8\n\t" \
    "s_add_u32 s46, s46, s52\n\t" \
    "s_add_u32 m0, m0, s53\n\t" \
    "v_exp_f32 v252, v252\n\t" \
    "v_exp_f32 v253, v253\n\t" \
    "v_exp_f32 v254, v254\n\t" \
    "v_exp_f32 v255, v255\n\t" \
    "v_exp_f32 v224, v224\n\t" \
    "v_exp_f32 v225, v225\n\t" \
    "v_smfmac_f32_32x32x32_bf16 v[64:79], v[196:199], v[248:255], v8\n\t" \
    "s_mov_b64 exec, s[44:45]\n\t" \
    "global_load_dwordx4 v[192:195], v10, s[40:41] offset:1024 sc1\n\t" \
    "global_load_dwordx4 v[196:199], v10, s[40:41] offset:1280 sc1\n\t" \
    "s_mov_b64 exec, -1\n\t" \
    "v_exp_f32 v226, v226\n\t" \
    "v_exp_f32 v227, v227\n\t" \
    "v_exp_f32 v228, v228\n\t" \
    "v_mfma_f32_32x32x16_bf16 v[240:255], v[180:183], v[24:27], 0\n\t" \
    "global_load_dwordx4 v[176:179], v11, s[40:41] offset:0 sc1\n\t" \
    "v_exp_f32 v229, v229\n\t" \
    "v_exp_f32 v230, v230\n\t" \
    "v_exp_f32 v231, v231\n\t" \
    "v_exp_f32 v232, v232\n\t" \
    "v_exp_f32 v233, v233\n\t" \
    "v_exp_f32 v234, v234\n\t" \
    "v_exp_f32 v235, v235\n\t" \
    "v_smfmac_f32_32x32x32_bf16 v[48:63], v[200:203], v[224:231], v8\n\t" \
    "v_exp_f32 v236, v236\n\t" \
    "v_exp_f32 v237, v237\n\t" \
    "v_exp_f32 v238, v238\n\t" \
    "v_exp_f32 v239, v239\n\t" \
    "v_exp_f32 v240, v240\n\t" \
    "v_exp_f32 v241, v241\n\t" \
    "v_smfmac_f32_32x32x32_bf16 v[48:63], v[204:207], v[232:239], v8\n\t" \
    "v_exp_f32 v242, v242\n\t" \
    "v_exp_f32 v243, v243\n\t" \
    "v_exp_f32 v244, v244\n\t" \
    "s_waitcnt vmcnt(7)\n\t" \
    "v_mfma_f32_32x32x16_bf16 v[224:239], v[184:187], v[28:31], 0\n\t" \
    "s_cbranch_vccnz 5f\n\t" \
    "6:\n\t" \
    "v_exp_f32 v245, v245\n\t" \
    "v_exp_f32 v246, v246\n\t" \
    "v_exp_f32 v247, v247\n\t" \
    "v_exp_f32 v248, v248\n\t" \
    "v_exp_f32 v249, v249\n\t" \
    "v_exp_f32 v250, v250\n\t" \
    "v_exp_f32 v251, v251\n\t" \
    "v_smfmac_f32_32x32x32_bf16 v[64:79], v[200:203], v[240:247], v8\n\t" \
    "v_exp_f32 v252, v252\n\t" \
    "v_exp_f32 v253, v253\n\t" \
    "v_exp_f32 v254, v254\n\t" \
    "v_exp_f32 v255, v255\n\t" \
    "v_exp_f32 v224, v224\n\t" \
    "v_exp_f32 v225, v225\n\t" \
    "v_smfmac_f32_32x32x32_bf16 v[64:79], v[204:207], v[248:255], v8\n\t" \
    "s_mov_b64 exec, s[44:45]\n\t" \
    "global_load_dwordx4 v[200:203], v10, s[40:41] offset:3072 sc1\n\t" \
    "global_load_dwordx4 v[204:207], v10, s[40:41] offset:3328 sc1\n\t" \
    "s_mov_b64 exec, -1\n\t" \
    "v_exp_f32 v226, v226\n\t" \
    "v_exp_f32 v227, v227\n\t" \
    "v_exp_f32 v228, v228\n\t" \
    "v_mfma_f32_32x32x16_bf16 v[240:255], v[184:187], v[24:27], 0\n\t" \
    "global_load_dwordx4 v[180:183], v11, s[40:41] offset:2048 sc1\n\t" \
    "v_exp_f32 v229, v229\n\t" \
    "v_exp_f32 v230, v230\n\t" \
    "v_exp_f32 v231, v231\n\t" \
    "v_exp_f32 v232, v232\n\t" \
    "v_exp_f32 v233, v233\n\t" \
    "v_exp_f32 v234, v234\n\t" \
    "v_exp_f32 v235, v235\n\t" \
    "v_smfmac_f32_32x32x32_bf16 v[48:63], v[208:211], v[224:231], v8\n\t" \
    "buffer_load_dwordx4 v12, s[76:79], s46 offen lds\n\t" \
    "v_exp_f32 v236, v236\n\t" \
    "v_exp_f32 v237, v237\n\t" \
    "v_exp_f32 v238, v238\n\t" \
    "v_exp_f32 v239, v239\n\t" \
    "v_exp_f32 v240, v240\n\t" \
    "v_exp_f32 v241, v241\n\t" \
    "v_smfmac_f32_32x32x32_bf16 v[48:63], v[212:215], v[232:239], v8\n\t" \
    "s_addk_i32 s40, 0x1000\n\t" \
    "s_bitset0_b32 s40, s48\n\t" \
    "s_min_u32 s40, s40, s54\n\t" \
    "v_exp_f32 v242, v242\n\t" \
    "v_exp_f32 v243, v243\n\t" \
    "v_exp_f32 v244, v244\n\t" \
    "s_waitcnt vmcnt(8)\n\t" \
    "v_mfma_f32_32x32x16_bf16 v[224:239], v[188:191], v[28:31], 0\n\t" \
    "v_exp_f32 v245, v245\n\t" \
    "v_exp_f32 v246, v246\n\t" \
    "v_exp_f32 v247, v247\n\t" \
    "v_exp_f32 v248, v248\n\t" \
    "v_exp_f32 v249, v249\n\t" \
    "v_exp_f32 v250, v250\n\t" \
    "v_exp_f32 v251, v251\n\t" \
    "v_smfmac_f32_32x32x32_bf16 v[64:79], v[208:211], v[240:247], v8\n\t" \
    "s_add_u32 s46, s46, s52\n\t" \
    "s_add_u32 m0, m0, s53\n\t" \
    "v_exp_f32 v252, v252\n\t" \
    "v_exp_f32 v253, v253\n\t" \
    "v_exp_f32 v254, v254\n\t" \
    "v_exp_f32 v255, v255\n\t" \
    "v_exp_f32 v224, v224\n\t" \
    "v_exp_f32 v225, v225\n\t" \
    "v_smfmac_f32_32x32x32_bf16 v[64:79], v[212:215], v[248:255], v8\n\t" \
    "s_mov_b64 exec, s[44:45]\n\t" \
    "global_load_dwordx4 v[208:211], v10, s[40:41] offset:1024 sc1\n\t" \
    "global_load_dwordx4 v[212:215], v10, s[40:41] offset:1280 sc1\n\t" \
    "s_mov_b64 exec, -1\n\t" \
    "v_exp_f32 v226, v226\n\t" \
    "v_exp_f32 v227, v227\n\t" \
    "v_exp_f32 v228, v228\n\t" \
    "v_mfma_f32_32x32x16_bf16 v[240:255], v[188:191], v[24:27], 0\n\t" \
    "global_load_dwordx4 v[184:187], v11, s[40:41] offset:0 sc1\n\t" \
    "v_exp_f32 v229, v229\n\t" \
    "v_exp_f32 v230, v230\n\t" \
    "v_exp_f32 v231, v231\n\t" \
    "v_exp_f32 v232, v232\n\t" \
    "v_exp_f32 v233, v233\n\t" \
    "v_exp_f32 v234, v234\n\t" \
    "v_exp_f32 v235, v235\n\t" \
    "v_smfmac_f32_32x32x32_bf16 v[48:63], v[216:219], v[224:231], v8\n\t" \
    "v_exp_f32 v236, v236\n\t" \
    "v_exp_f32 v237, v237\n\t" \
    "v_exp_f32 v238, v238\n\t" \
    "v_exp_f32 v239, v239\n\t" \
    "v_exp_f32 v240, v240\n\t" \
    "v_exp_f32 v241, v241\n\t" \
    "v_smfmac_f32_32x32x32_bf16 v[48:63], v[220:223], v[232:239], v8\n\t" \
    "s_add_i32 s47, s47, -1\n\t" \
    "v_exp_f32 v242, v242\n\t" \
    "v_exp_f32 v243, v243\n\t" \
    "v_exp_f32 v244, v244\n\t" \
    "s_waitcnt vmcnt(7)\n\t" \
    "v_mfma_f32_32x32x16_bf16 v[224:239], v[176:179], v[28:31], 0\n\t" \
    "v_exp_f32 v245, v245\n\t" \
    "v_exp_f32 v246, v246\n\t" \
    "v_exp_f32 v247, v247\n\t" \
    "v_exp_f32 v248, v248\n\t" \
    "v_exp_f32 v249, v249\n\t" \
    "v_exp_f32 v250, v250\n\t" \
    "v_exp_f32 v251, v251\n\t" \
    "v_smfmac_f32_32x32x32_bf16 v[64:79], v[216:219], v[240:247], v8\n\t" \
    "s_cmp_eq_u32 s47, 0\n\t" \
    "v_exp_f32 v252, v252\n\t" \
    "v_exp_f32 v253, v253\n\t" \
    "v_exp_f32 v254, v254\n\t" \
    "v_exp_f32 v255, v255\n\t" \
    "s_cbranch_scc0 1b\n\t" \
    "s_nop 1\n\t" \
    "v_smfmac_f32_32x32x32_bf16 v[64:79], v[220:223], v[248:255], v8\n\t" \
      \
    "v_mfma_f32_32x32x16_bf16 v[224:239], v[176:179], v[20:23], v[96:111]\n\t" \
    "s_mov_b64 exec, s[44:45]\n\t" \
    "global_load_dwordx4 v[216:219], v10, s[40:41] offset:3072 sc1\n\t" \
    "global_load_dwordx4 v[220:223], v10, s[40:41] offset:3328 sc1\n\t" \
    "s_mov_b64 exec, -1\n\t" \
    "global_load_dwordx4 v[188:191], v11, s[40:41] offset:2048 sc1\n\t" \
    "s_nop 6\n\t" \
    "v_exp_f32 v224, v224\n\t" \
    "v_exp_f32 v225, v225\n\t" \
    "v_exp_f32 v226, v226\n\t" \
    "v_exp_f32 v227, v227\n\t" \
    "v_exp_f32 v228, v228\n\t" \
    "s_waitcnt vmcnt(7)\n\t" \
    "v_mfma_f32_32x32x16_bf16 v[240:255], v[180:183], v[20:23], 0\n\t" \
    "v_exp_f32 v229, v229\n\t" \
    "v_exp_f32 v230, v230\n\t" \
    "v_exp_f32 v231, v231\n\t" \
    "v_exp_f32 v232, v232\n\t" \
    "v_exp_f32 v233, v233\n\t" \
    "v_exp_f32 v234, v234\n\t" \
    "v_exp_f32 v235, v235\n\t" \
    "v_smfmac_f32_32x32x32_bf16 v[80:95], v[192:195], v[224:231], v8\n\t" \
    "s_addk_i32 s40, 0x1000\n\t" \
    "s_min_u32 s40, s40, s54\n\t" \
    "s_cmp_eq_u32 s73, 1\n\t" \
    "v_exp_f32 v236, v236\n\t" \
    "v_exp_f32 v237, v237\n\t" \
    "v_exp_f32 v238, v238\n\t" \
    "v_exp_f32 v239, v239\n\t" \
    "v_exp_f32 v240, v240\n\t" \
    "v_exp_f32 v241, v241\n\t" \
    "v_smfmac_f32_32x32x32_bf16 v[80:95], v[196:199], v[232:239], v8\n\t" \
    "s_cbranch_scc1 80f\n\t" \
    "s_mov_b64 exec, s[44:45]\n\t" \
    "global_load_dwordx4 v[192:195], v10, s[40:41] offset:1024 sc1\n\t" \
    "global_load_dwordx4 v[196:199], v10, s[40:41] offset:1280 sc1\n\t" \
    "s_mov_b64 exec, -1\n\t" \
    "v_exp_f32 v242, v242\n\t" \
    "v_exp_f32 v243, v243\n\t" \
    "v_exp_f32 v244, v244\n\t" \
    "s_waitcnt vmcnt(5)\n\t" \
    "v_mfma_f32_32x32x16_bf16 v[224:239], v[184:187], v[20:23], 0\n\t" \
    "global_load_dwordx4 v[176:179], v11, s[40:41] offset:0 sc1\n\t" \
    "v_exp_f32 v245, v245\n\t" \
    "v_exp_f32 v246, v246\n\t" \
    "v_exp_f32 v247, v247\n\t" \
    "v_exp_f32 v248, v248\n\t" \
    "v_exp_f32 v249, v249\n\t" \
    "v_exp_f32 v250, v250\n\t" \
    "v_exp_f32 v251, v251\n\t" \
    "v_smfmac_f32_32x32x32_bf16 v[80:95], v[200:203], v[240:247], v8\n\t" \
    "s_cmp_eq_u32 s73, 2\n\t" \
    "v_exp_f32 v252, v252\n\t" \
    "v_exp_f32 v253, v253\n\t" \
    "v_exp_f32 v254, v254\n\t" \
    "v_exp_f32 v255, v255\n\t" \
    "v_exp_f32 v224, v224\n\t" \
    "v_exp_f32 v225, v225\n\t" \
    "v_smfmac_f32_32x32x32_bf16 v[80:95], v[204:207], v[248:255], v8\n\t" \
    "s_cbranch_scc1 80f\n\t" \
    "s_mov_b64 exec, s[44:45]\n\t" \
    "global_load_dwordx4 v[200:203], v10, s[40:41] offset:3072 sc1\n\t" \
    "global_load_dwordx4 v[204:207], v10, s[40:41] offset:3328 sc1\n\t" \
    "s_mov_b64 exec, -1\n\t" \
    "v_exp_f32 v226, v226\n\t" \
    "v_exp_f32 v227, v227\n\t" \
    "v_exp_f32 v228, v228\n\t" \
    "s_waitcnt vmcnt(5)\n\t" \
    "v_mfma_f32_32x32x16_bf16 v[240:255], v[188:191], v[20:23], 0\n\t" \
    "global_load_dwordx4 v[180:183], v11, s[40:41] offset:2048 sc1\n\t" \
    "v_exp_f32 v229, v229\n\t" \
    "v_exp_f32 v230, v230\n\t" \
    "v_exp_f32 v231, v231\n\t" \
    "v_exp_f32 v232, v232\n\t" \
    "v_exp_f32 v233, v233\n\t" \
    "v_exp_f32 v234, v234\n\t" \
    "v_exp_f32 v235, v235\n\t" \
    "v_smfmac_f32_32x32x32_bf16 v[80:95], v[208:211], v[224:231], v8\n\t" \
    "s_addk_i32 s40, 0x1000\n\t" \
    "s_min_u32 s40, s40, s54\n\t" \
    "s_cmp_eq_u32 s73, 3\n\t" \
    "v_exp_f32 v236, v236\n\t" \
    "v_exp_f32 v237, v237\n\t" \
    "v_exp_f32 v238, v238\n\t" \
    "v_exp_f32 v239, v239\n\t" \
    "v_exp_f32 v240, v240\n\t" \
    "v_exp_f32 v241, v241\n\t" \
    "v_smfmac_f32_32x32x32_bf16 v[80:95], v[212:215], v[232:239], v8\n\t" \
    "s_cbranch_scc1 80f\n\t" \
    "s_mov_b64 exec, s[44:45]\n\t" \
    "global_load_dwordx4 v[208:211], v10, s[40:41] offset:1024 sc1\n\t" \
    "global_load_dwordx4 v[212:215], v10, s[40:41] offset:1280 sc1\n\t" \
    "s_mov_b64 exec, -1\n\t" \
    "v_exp_f32 v242, v242\n\t" \
    "v_exp_f32 v243, v243\n\t" \
    "v_exp_f32 v244, v244\n\t" \
    "s_waitcnt vmcnt(5)\n\t" \
    "v_mfma_f32_32x32x16_bf16 v[224:239], v[176:179], v[20:23], 0\n\t" \
    "global_load_dwordx4 v[184:187], v11, s[40:41] offset:0 sc1\n\t" \
    "v_exp_f32 v245, v245\n\t" \
    "v_exp_f32 v246, v246\n\t" \
    "v_exp_f32 v247, v247\n\t" \
    "v_exp_f32 v248, v248\n\t" \
    "v_exp_f32 v249, v249\n\t" \
    "v_exp_f32 v250, v250\n\t" \
    "v_exp_f32 v251, v251\n\t" \
    "v_smfmac_f32_32x32x32_bf16 v[80:95], v[216:219], v[240:247], v8\n\t" \
    "s_cmp_eq_u32 s73, 4\n\t" \
    "v_exp_f32 v252, v252\n\t" \
    "v_exp_f32 v253, v253\n\t" \
    "v_exp_f32 v254, v254\n\t" \
    "v_exp_f32 v255, v255\n\t" \
    "v_exp_f32 v224, v224\n\t" \
    "v_exp_f32 v225, v225\n\t" \
    "v_smfmac_f32_32x32x32_bf16 v[80:95], v[220:223], v[248:255], v8\n\t" \
    "s_cbranch_scc1 80f\n\t" \
    "s_mov_b64 exec, s[44:45]\n\t" \
    "global_load_dwordx4 v[216:219], v10, s[40:41] offset:3072 sc1\n\t" \
    "global_load_dwordx4 v[220:223], v10, s[40:41] offset:3328 sc1\n\t" \
    "s_mov_b64 exec, -1\n\t" \
    "v_exp_f32 v226, v226\n\t" \
    "v_exp_f32 v227, v227\n\t" \
    "v_exp_f32 v228, v228\n\t" \
    "s_waitcnt vmcnt(5)\n\t" \
    "v_mfma_f32_32x32x16_bf16 v[240:255], v[180:183], v[20:23], 0\n\t" \
    "global_load_dwordx4 v[188:191], v11, s[40:41] offset:2048 sc1\n\t" \
    "v_exp_f32 v229, v229\n\t" \
    "v_exp_f32 v230, v230\n\t" \
    "v_exp_f32 v231, v231\n\t" \
    "v_exp_f32 v232, v232\n\t" \
    "v_exp_f32 v233, v233\n\t" \
    "v_exp_f32 v234, v234\n\t" \
    "v_exp_f32 v235, v235\n\t" \
    "v_smfmac_f32_32x32x32_bf16 v[80:95], v[192:195], v[224:231], v8\n\t" \
    "s_addk_i32 s40, 0x1000\n\t" \
    "s_min_u32 s40, s40, s54\n\t" \
    "s_cmp_eq_u32 s73, 5\n\t" \
    "v_exp_f32 v236, v236\n\t" \
    "v_exp_f32 v237, v237\n\t" \
    "v_exp_f32 v238, v238\n\t" \
    "v_exp_f32 v239, v239\n\t" \
    "v_exp_f32 v240, v240\n\t" \
    "v_exp_f32 v241, v241\n\t" \
    "v_smfmac_f32_32x32x32_bf16 v[80:95], v[196:199], v[232:239], v8\n\t" \
    "s_cbranch_scc1 80f\n\t" \
    "s_mov_b64 exec, s[44:45]\n\t" \
    "global_load_dwordx4 v[192:195], v10, s[40:41] offset:1024 sc1\n\t" \
    "global_load_dwordx4 v[196:199], v10, s[40:41] offset:1280 sc1\n\t" \
    "s_mov_b64 exec, -1\n\t" \
    "v_exp_f32 v242, v242\n\t" \
    "v_exp_f32 v243, v243\n\t" \
    "v_exp_f32 v244, v244\n\t" \
    "s_waitcnt vmcnt(5)\n\t" \
    "v_mfma_f32_32x32x16_bf16 v[224:239], v[184:187], v[20:23], 0\n\t" \
    "global_load_dwordx4 v[176:179], v11, s[40:41] offset:0 sc1\n\t" \
    "v_exp_f32 v245, v245\n\t" \
    "v_exp_f32 v246, v246\n\t" \
    "v_exp_f32 v247, v247\n\t" \
    "v_exp_f32 v248, v248\n\t" \
    "v_exp_f32 v249, v249\n\t" \
    "v_exp_f32 v250, v250\n\t" \
    "v_exp_f32 v251, v251\n\t" \
    "v_smfmac_f32_32x32x32_bf16 v[80:95], v[200:203], v[240:247], v8\n\t" \
    "s_xor_b32 s40, s70, 0x8000\n\t" \
    "s_mov_b32 s48, 16\n\t" \
    "s_cmp_eq_u32 s73, 6\n\t" \
    "v_exp_f32 v252, v252\n\t" \
    "v_exp_f32 v253, v253\n\t" \
    "v_exp_f32 v254, v254\n\t" \
    "v_exp_f32 v255, v255\n\t" \
    "v_exp_f32 v224, v224\n\t" \
    "v_exp_f32 v225, v225\n\t" \
    "v_smfmac_f32_32x32x32_bf16 v[80:95], v[204:207], v[248:255], v8\n\t" \
    "s_cbranch_scc1 80f\n\t" \
    "s_mov_b64 exec, s[44:45]\n\t" \
    "global_load_dwordx4 v[200:203], v10, s[40:41] offset:1024 sc1\n\t" \
    "global_load_dwordx4 v[204:207], v10, s[40:41] offset:1280 sc1\n\t" \
    "s_mov_b64 exec, -1\n\t" \
    "v_exp_f32 v226, v226\n\t" \
    "v_exp_f32 v227, v227\n\t" \
    "v_exp_f32 v228, v228\n\t" \
    "s_waitcnt vmcnt(5)\n\t" \
    "v_mfma_f32_32x32x16_bf16 v[240:255], v[188:191], v[20:23], 0\n\t" \
    "global_load_dwordx4 v[180:183], v11, s[40:41] offset:0 sc1\n\t" \
    "v_exp_f32 v229, v229\n\t" \
    "v_exp_f32 v230, v230\n\t" \
    "v_exp_f32 v231, v231\n\t" \
    "v_exp_f32 v232, v232\n\t" \
    "v_exp_f32 v233, v233\n\t" \
    "v_exp_f32 v234, v234\n\t" \
    "v_exp_f32 v235, v235\n\t" \
    "v_smfmac_f32_32x32x32_bf16 v[80:95], v[208:211], v[224:231], v8\n\t" \
    "s_cmp_eq_u32 s73, 7\n\t" \
    "v_exp_f32 v236, v236\n\t" \
    "v_exp_f32 v237, v237\n\t" \
    "v_exp_f32 v238, v238\n\t" \
    "v_exp_f32 v239, v239\n\t" \
    "v_exp_f32 v240, v240\n\t" \
    "v_exp_f32 v241, v241\n\t" \
    "v_smfmac_f32_32x32x32_bf16 v[80:95], v[212:215], v[232:239], v8\n\t" \
    "s_cbranch_scc1 81f\n\t" \
    "s_mov_b64 exec, s[44:45]\n\t" \
    "global_load_dwordx4 v[208:211], v10, s[40:41] offset:3072 sc1\n\t" \
    "global_load_dwordx4 v[212:215], v10, s[40:41] offset:3328 sc1\n\t" \
    "s_mov_b64 exec, -1\n\t" \
    "v_exp_f32 v242, v242\n\t" \
    "v_exp_f32 v243, v243\n\t" \
    "v_exp_f32 v244, v244\n\t" \
    "s_waitcnt vmcnt(5)\n\t" \
    "v_mfma_f32_32x32x16_bf16 v[224:239], v[176:179], v[20:23], v[112:127]\n\t" \
    "global_load_dwordx4 v[184:187], v11, s[40:41] offset:2048 sc1\n\t" \
    "v_exp_f32 v245, v245\n\t" \
    "v_exp_f32 v246, v246\n\t" \
    "v_exp_f32 v247, v247\n\t" \
    "v_exp_f32 v248, v248\n\t" \
    "v_exp_f32 v249, v249\n\t" \
    "v_exp_f32 v250, v250\n\t" \
    "v_exp_f32 v251, v251\n\t" \
    "v_smfmac_f32_32x32x32_bf16 v[80:95], v[216:219], v[240:247], v8\n\t" \
    "s_addk_i32 s40, 0x1000\n\t" \
    "s_bitset0_b32 s40, s48\n\t" \
    "s_cmp_eq_u32 s73, 8\n\t" \
    "v_exp_f32 v252, v252\n\t" \
    "v_exp_f32 v253, v253\n\t" \
    "v_exp_f32 v254, v254\n\t" \
    "v_exp_f32 v255, v255\n\t" \
    "v_exp_f32 v224, v224\n\t" \
    "v_exp_f32 v225, v225\n\t" \
    "v_smfmac_f32_32x32x32_bf16 v[80:95], v[220:223], v[248:255], v8\n\t" \
    "s_cbranch_scc1 82f\n\t" \
    "s_mov_b64 exec, s[44:45]\n\t" \
    "global_load_dwordx4 v[216:219], v10, s[40:41] offset:1024 sc1\n\t" \
    "global_load_dwordx4 v[220:223], v10, s[40:41] offset:1280 sc1\n\t" \
    "s_mov_b64 exec, -1\n\t" \
    "v_exp_f32 v226, v226\n\t" \
    "v_exp_f32 v227, v227\n\t" \
    "v_exp_f32 v228, v228\n\t" \
    "global_load_dwordx4 v[188:191], v11, s[40:41] offset:0 sc1\n\t" \
    "v_exp_f32 v229, v229\n\t" \
    "v_exp_f32 v230, v230\n\t" \
    "v_exp_f32 v231, v231\n\t" \
    "v_exp_f32 v232, v232\n\t" \
    "v_exp_f32 v233, v233\n\t" \
    "v_exp_f32 v234, v234\n\t" \
    "v_exp_f32 v235, v235\n\t" \
    "v_smfmac_f32_32x32x32_bf16 v[80:95], v[192:195], v[224:231], v8\n\t" \
    "v_exp_f32 v236, v236\n\t" \
    "v_exp_f32 v237, v237\n\t" \
    "v_exp_f32 v238, v238\n\t" \
    "v_exp_f32 v239, v239\n\t" \
    "s_nop 1\n\t" \
    "v_smfmac_f32_32x32x32_bf16 v[80:95], v[196:199], v[232:239], v8\n\t" \
    "s_branch 9f\n\t" \
      \
    "3:\n\t" \
    "s_cmp_eq_u32 s47, 1\n\t" \
    "s_cbranch_scc1 7f\n\t" \
    "global_load_dwordx2 v[32:33], v15, s[68:69] sc1\n\t" \
    "ds_read_b128 v[0:3], v7 offset:8192\n\t" \
    "ds_read_b128 v[4:7], v7 offset:16384\n\t" \
    "ds_read2_b32 v[96:97], v13 offset0:0 offset1:1\n\t" \
    "ds_read2_b32 v[98:99], v13 offset0:2 offset1:3\n\t" \
    "ds_read2_b32 v[100:101], v13 offset0:8 offset1:9\n\t" \
    "ds_read2_b32 v[102:103], v13 offset0:10 offset1:11\n\t" \
    "ds_read2_b32 v[104:105], v13 offset0:16 offset1:17\n\t" \
    "ds_read2_b32 v[106:107], v13 offset0:18 offset1:19\n\t" \
    "ds_read2_b32 v[108:109], v13 offset0:24 offset1:25\n\t" \
    "ds_read2_b32 v[110:111], v13 offset0:26 offset1:27\n\t" \
    "v_mov_b64 v[34:35], 0\n\t" \
    "s_waitcnt lgkmcnt(8)\n\t" \
    "ds_read2st64_b32 v[16:17], v7 offset0:0 offset1:1\n\t" \
    "v_mfma_f32_32x32x8bf16_1k v[80:95], v[34:35], v[34:35], 0\n\t" \
    "ds_read2st64_b32 v[18:19], v7 offset0:2 offset1:3\n\t" \
    "ds_read2_b32 v[112:113], v13 offset0:64 offset1:65\n\t" \
    "ds_read2_b32 v[114:115], v13 offset0:66 offset1:67\n\t" \
    "ds_read2_b32 v[116:117], v13 offset0:72 offset1:73\n\t" \
    "ds_read2_b32 v[118:119], v13 offset0:74 offset1:75\n\t" \
    "ds_read2_b32 v[120:121], v13 offset0:80 offset1:81\n\t" \
    "ds_read2_b32 v[122:123], v13 offset0:82 offset1:83\n\t" \
    "ds_read2_b32 v[124:125], v13 offset0:88 offset1:89\n\t" \
    "ds_read2_b32 v[126:127], v13 offset0:90 offset1:91\n\t" \
    "s_branch 4b\n\t" \
      \
    "7:\n\t" \
    "s_waitcnt lgkmcnt(0)\n\t" \
    "v_pk_add_f32 v[16:17], v[16:17], v[18:19]\n\t" \
    "s_add_u32 s40, s72, 0xfffff000\n\t" \
    "v_add_f32 v16, v16, v17\n\t" \
    "s_mov_b32 s48, 0\n\t" \
    "s_lshl_b32 s54, s73, 11\n\t" \
    "s_add_u32 s54, s54, s40\n\t" \
    "s_addk_i32 s54, 0x800\n\t" \
    "v_mul_f32 v16, 0xbfb8aa3b, v16\n\t" \
    "v_exp_f32 v16, v16\n\t" \
    "v_cmp_ne_u64_e64 s[42:43], s[36:37], v[32:33]\n\t" \
    "s_cmp_lg_u64 s[42:43], 0\n\t" \
    "s_cbranch_scc1 210f\n\t" \
    "212:\n\t" \
    "v_add_f32 v16, 1.0, v16\n\t" \
    "v_rcp_f32 v14, v16\n\t" \
    "s_branch 4b\n\t" \
      \
    "210:\n\t" \
    "s_mov_b32 s50, 0x10000\n\t" \
    "211:\n\t" \
    "s_sleep 2\n\t" \
    "global_load_dwordx2 v[32:33], v15, s[68:69] sc1\n\t" \
    "s_add_i32 s50, s50, -1\n\t" \
    "s_cmp_eq_u32 s50, 0\n\t" \
    "s_cbranch_scc1 212b\n\t" \
    "s_waitcnt vmcnt(0)\n\t" \
    "v_cmp_ne_u64_e64 s[42:43], s[36:37], v[32:33]\n\t" \
    "s_cmp_lg_u64 s[42:43], 0\n\t" \
    "s_cbranch_scc1 211b\n\t" \
    "s_branch 212b\n\t" \
      \
    "200:\n\t" \
    "s_mov_b32 s50, 0x10000\n\t" \
    "201:\n\t" \
    "s_sleep 2\n\t" \
    "global_load_dwordx2 v[188:189], v14, s[66:67] sc1\n\t" \
    "s_add_i32 s50, s50, -1\n\t" \
    "s_cmp_eq_u32 s50, 0\n\t" \
    "s_cbranch_scc1 202b\n\t" \
    "s_waitcnt vmcnt(0)\n\t" \
    "v_cmp_ne_u64_e64 s[42:43], s[36:37], v[188:189]\n\t" \
    "s_cmp_lg_u64 s[42:43], 0\n\t" \
    "s_cbranch_scc1 201b\n\t" \
    "s_branch 202b\n\t" \
      \
    "52:\n\t" \
    "s_waitcnt vmcnt(6)\n\t" \
    "s_mov_b64 exec, 1\n\t" \
    "v_mov_b32 v16, 133636\n\t" \
    "v_mov_b32 v17, 1\n\t" \
    "ds_add_rtn_u32 v17, v16, v17\n\t" \
    "v_mov_b64 v[18:19], s[36:37]\n\t" \
    "s_waitcnt lgkmcnt(0)\n\t" \
    "v_readfirstlane_b32 s50, v17\n\t" \
    "s_cmp_lg_u32 s50, 7\n\t" \
    "s_cbranch_scc1 54f\n\t" \
    "global_store_dwordx2 v9, v[18:19], s[64:65] offset:2048 sc1\n\t" \
    "54:\n\t" \
    "s_mov_b64 exec, -1\n\t" \
    "s_load_dword s51, s[62:63], 0\n\t" \
    "s_load_dword s51, s[62:63], 64\n\t" \
    "s_load_dword s51, s[62:63], 128\n\t" \
    "s_load_dword s51, s[62:63], 192\n\t" \
    "s_load_dword s51, s[62:63], 256\n\t" \
    "s_load_dword s51, s[62:63], 320\n\t" \
    "s_load_dword s51, s[62:63], 384\n\t" \
    "s_load_dword s51, s[62:63], 448\n\t" \
    "s_mov_b64 vcc, 0\n\t" \
    "s_setprio 3\n\t" \
    "s_branch 53b\n\t" \
    "5:\n\t" \
    "s_mov_b64 vcc, 0\n\t" \
    "s_setprio 3\n\t" \
    "s_mov_b64 exec, 1\n\t" \
    "v_mov_b32 v16, 133636\n\t" \
    "v_mov_b32 v17, 1\n\t" \
    "ds_add_rtn_u32 v17, v16, v17\n\t" \
    "v_mov_b64 v[18:19], s[36:37]\n\t" \
    "s_waitcnt lgkmcnt(0)\n\t" \
    "v_readfirstlane_b32 s50, v17\n\t" \
    "s_cmp_lg_u32 s50, 7\n\t" \
    "s_cbranch_scc1 55f\n\t" \
    "global_store_dwordx2 v9, v[18:19], s[64:65] offset:2048 sc1\n\t" \
    "55:\n\t" \
    "s_mov_b64 exec, -1\n\t" \
    "s_load_dword s51, s[62:63], 0\n\t" \
    "s_load_dword s51, s[62:63], 64\n\t" \
    "s_load_dword s51, s[62:63], 128\n\t" \
    "s_load_dword s51, s[62:63], 192\n\t" \
    "s_load_dword s51, s[62:63], 256\n\t" \
    "s_load_dword s51, s[62:63], 320\n\t" \
    "s_load_dword s51, s[62:63], 384\n\t" \
    "s_load_dword s51, s[62:63], 448\n\t" \
    "s_branch 6b\n\t" \
      \
    "80:\n\t" \
    "s_xor_b32 s40, s70, 0x8000\n\t" \
    "s_mov_b32 s48, 16\n\t" \
    "global_load_dwordx4 v[180:183], v11, s[40:41] offset:0 sc1\n\t" \
    "s_mov_b64 exec, s[44:45]\n\t" \
    "global_load_dwordx4 v[200:203], v10, s[40:41] offset:1024 sc1\n\t" \
    "global_load_dwordx4 v[204:207], v10, s[40:41] offset:1280 sc1\n\t" \
    "s_mov_b64 exec, -1\n\t" \
    "81:\n\t" \
    "global_load_dwordx4 v[184:187], v11, s[40:41] offset:2048 sc1\n\t" \
    "s_mov_b64 exec, s[44:45]\n\t" \
    "global_load_dwordx4 v[208:211], v10, s[40:41] offset:3072 sc1\n\t" \
    "global_load_dwordx4 v[212:215], v10, s[40:41] offset:3328 sc1\n\t" \
    "s_mov_b64 exec, -1\n\t" \
    "s_addk_i32 s40, 0x1000\n\t" \
    "s_bitset0_b32 s40, s48\n\t" \
    "82:\n\t" \
    "global_load_dwordx4 v[188:191], v11, s[40:41] offset:0 sc1\n\t" \
    "s_mov_b64 exec, s[44:45]\n\t" \
    "global_load_dwordx4 v[216:219], v10, s[40:41] offset:1024 sc1\n\t" \
    "global_load_dwordx4 v[220:223], v10, s[40:41] offset:1280 sc1\n\t" \
    "s_mov_b64 exec, -1\n\t" \
    "9:\n\t" \
    "s_nop 10\n\t" \
      \
    "v_pk_mul_f32 v[16:17], v[80:81], 0\n\t" \
    "v_pk_fma_f32 v[16:17], v[82:83], 0, v[16:17]\n\t" \
    "v_pk_fma_f32 v[16:17], v[84:85], 0, v[16:17]\n\t" \
    "v_pk_fma_f32 v[16:17], v[86:87], 0, v[16:17]\n\t" \
    "v_add_f32 v16, v16, v17\n\t" \
    "v_add_f32 v16, v16, v88\n\t" \
    "v_subrev_u32 v16, 0x2b800000, v16\n\t" \
    "v_cmp_lt_u32_e32 vcc, 0x41000000, v16\n\t" \
    "s_mov_b64 s[38:39], vcc\n\t" \
    ""
#define PART2_ASM \
    "s_mov_b32 s41, s71\n\t" \
    "s_mov_b32 s44, 0xffff\n\t" \
    "s_mov_b32 s45, 0xffff\n\t" \
    "s_xor_b32 s40, s70, 0x8000\n\t" \
    "s_mov_b32 s48, 16\n\t" \
    "s_addk_i32 s40, 0x1000\n\t" \
    "s_bitset0_b32 s40, s48\n\t" \
    "s_abs_i32 s46, s75\n\t" \
    "s_ashr_i32 s52, s75, 31\n\t" \
    "s_lshr_b32 s53, s46, 3\n\t" \
    "s_or_b32 s52, s52, 1\n\t" \
    "s_add_u32 m0, s53, 0\n\t" \
    "s_lshl_b32 s53, s52, 10\n\t" \
    "s_lshl_b32 s52, s52, 13\n\t" \
      \
    "s_waitcnt vmcnt(6)\n\t" \
    "v_mfma_f32_32x32x16_bf16 v[224:239], v[180:183], v[24:27], 0\n\t" \
    "v_mov_b64 v[20:21], -1\n\t" \
    "v_mov_b64 v[22:23], -1\n\t" \
    "v_sub_f32 v13, 1.0, v14\n\t" \
    "s_xor_b32 s50, s60, 2099200\n\t" \
    "buffer_store_dwordx4 v[20:23], v6, s[56:59], s60 offen sc1\n\t" \
    "v_rcp_f32 v88, v88\n\t" \
    "s_nop 0\n\t" \
    "v_mul_f32 v88, v13, v88\n\t" \
    "v_pk_mul_f32 v[80:81], v[80:81], v[88:89] op_sel_hi:[1,0]\n\t" \
    "v_pk_mul_f32 v[82:83], v[82:83], v[88:89] op_sel_hi:[1,0]\n\t" \
    "v_pk_mul_f32 v[84:85], v[84:85], v[88:89] op_sel_hi:[1,0]\n\t" \
    "v_pk_mul_f32 v[86:87], v[86:87], v[88:89] op_sel_hi:[1,0]\n\t" \
    "v_cvt_pk_bf16_f32 v16, v80, v81\n\t" \
    "v_cvt_pk_bf16_f32 v17, v82, v83\n\t" \
    "v_cvt_pk_bf16_f32 v18, v84, v85\n\t" \
    "v_cvt_pk_bf16_f32 v19, v86, v87\n\t" \
    "buffer_store_dwordx4 v[16:19], v6, s[56:59], s50 offen sc1\n\t" \
    "s_lshl_b32 s50, s74, 4\n\t" \
    "s_add_u32 s50, s50, 133768\n\t" \
    "v_mov_b32 v15, s50\n\t" \
    "s_add_u32 s49, s61, 0x200000\n\t" \
    "s_add_u32 s51, s61, 0x201000\n\t" \
    "s_mov_b64 vcc, -1\n\t" \
      \
    "s_mov_b32 s47, 3\n\t" \
    "v_exp_f32 v224, v224\n\t" \
    "v_exp_f32 v225, v225\n\t" \
    "s_branch 2f\n\t" \
    ".p2align 6\n\t" \
    "1:\n\t" \
    "v_exp_f32 v224, v224\n\t" \
    "v_exp_f32 v225, v225\n\t" \
    "v_smfmac_f32_32x32x32_bf16 v[48:63], v[196:199], v[248:255], v8\n\t" \
    "2:\n\t" \
    "s_mov_b64 exec, s[44:45]\n\t" \
    "global_load_dwordx4 v[192:195], v10, s[40:41] offset:3072 sc1\n\t" \
    "global_load_dwordx4 v[196:199], v10, s[40:41] offset:3328 sc1\n\t" \
    "s_mov_b64 exec, -1\n\t" \
    "v_exp_f32 v226, v226\n\t" \
    "v_exp_f32 v227, v227\n\t" \
    "v_exp_f32 v228, v228\n\t" \
    "v_mfma_f32_32x32x16_bf16 v[240:255], v[180:183], v[28:31], 0\n\t" \
    "global_load_dwordx4 v[176:179], v11, s[40:41] offset:2048 sc1\n\t" \
    "v_exp_f32 v229, v229\n\t" \
    "v_exp_f32 v230, v230\n\t" \
    "v_exp_f32 v231, v231\n\t" \
    "v_exp_f32 v232, v232\n\t" \
    "v_exp_f32 v233, v233\n\t" \
    "v_exp_f32 v234, v234\n\t" \
    "v_exp_f32 v235, v235\n\t" \
    "v_smfmac_f32_32x32x32_bf16 v[64:79], v[200:203], v[224:231], v8\n\t" \
    "buffer_load_dwordx4 v12, s[76:79], s46 offen lds\n\t" \
    "s_cmp_le_u32 s47, 1\n\t" \
    "s_cbranch_scc1 3f\n\t" \
    "4:\n\t" \
    "v_exp_f32 v236, v236\n\t" \
    "v_exp_f32 v237, v237\n\t" \
    "v_exp_f32 v238, v238\n\t" \
    "v_exp_f32 v239, v239\n\t" \
    "v_exp_f32 v240, v240\n\t" \
    "v_exp_f32 v241, v241\n\t" \
    "v_smfmac_f32_32x32x32_bf16 v[64:79], v[204:207], v[232:239], v8\n\t" \
    "s_addk_i32 s40, 0x1000\n\t" \
    "s_bitset0_b32 s40, s48\n\t" \
    "v_exp_f32 v242, v242\n\t" \
    "v_exp_f32 v243, v243\n\t" \
    "v_exp_f32 v244, v244\n\t" \
    "s_waitcnt vmcnt(8)\n\t" \
    "v_mfma_f32_32x32x16_bf16 v[224:239], v[184:187], v[24:27], 0\n\t" \
    "v_exp_f32 v245, v245\n\t" \
    "v_exp_f32 v246, v246\n\t" \
    "v_exp_f32 v247, v247\n\t" \
    "v_exp_f32 v248, v248\n\t" \
    "v_exp_f32 v249, v249\n\t" \
    "v_exp_f32 v250, v250\n\t" \
    "v_exp_f32 v251, v251\n\t" \
    "v_smfmac_f32_32x32x32_bf16 v[48:63], v[200:203], v[240:247], v8\n\t" \
    "s_add_u32 s46, s46, s52\n\t" \
    "s_add_u32 m0, m0, s53\n\t" \
    "v_exp_f32 v252, v252\n\t" \
    "v_exp_f32 v253, v253\n\t" \
    "v_exp_f32 v254, v254\n\t" \
    "v_exp_f32 v255, v255\n\t" \
    "v_exp_f32 v224, v224\n\t" \
    "v_exp_f32 v225, v225\n\t" \
    "v_smfmac_f32_32x32x32_bf16 v[48:63], v[204:207], v[248:255], v8\n\t" \
    "s_mov_b64 exec, s[44:45]\n\t" \
    "global_load_dwordx4 v[200:203], v10, s[40:41] offset:1024 sc1\n\t" \
    "global_load_dwordx4 v[204:207], v10, s[40:41] offset:1280 sc1\n\t" \
    "s_mov_b64 exec, -1\n\t" \
    "v_exp_f32 v226, v226\n\t" \
    "v_exp_f32 v227, v227\n\t" \
    "v_exp_f32 v228, v228\n\t" \
    "v_mfma_f32_32x32x16_bf16 v[240:255], v[184:187], v[28:31], 0\n\t" \
    "global_load_dwordx4 v[180:183], v11, s[40:41] offset:0 sc1\n\t" \
    "v_exp_f32 v229, v229\n\t" \
    "v_exp_f32 v230, v230\n\t" \
    "v_exp_f32 v231, v231\n\t" \
    "v_exp_f32 v232, v232\n\t" \
    "v_exp_f32 v233, v233\n\t" \
    "v_exp_f32 v234, v234\n\t" \
    "v_exp_f32 v235, v235\n\t" \
    "v_smfmac_f32_32x32x32_bf16 v[64:79], v[208:211], v[224:231], v8\n\t" \
    "v_exp_f32 v236, v236\n\t" \
    "v_exp_f32 v237, v237\n\t" \
    "v_exp_f32 v238, v238\n\t" \
    "v_exp_f32 v239, v239\n\t" \
    "v_exp_f32 v240, v240\n\t" \
    "v_exp_f32 v241, v241\n\t" \
    "v_smfmac_f32_32x32x32_bf16 v[64:79], v[212:215], v[232:239], v8\n\t" \
    "v_exp_f32 v242, v242\n\t" \
    "v_exp_f32 v243, v243\n\t" \
    "v_exp_f32 v244, v244\n\t" \
    "s_waitcnt vmcnt(7)\n\t" \
    "v_mfma_f32_32x32x16_bf16 v[224:239], v[188:191], v[24:27], 0\n\t" \
    "s_cbranch_vccnz 5f\n\t" \
    "6:\n\t" \
    "v_exp_f32 v245, v245\n\t" \
    "v_exp_f32 v246, v246\n\t" \
    "v_exp_f32 v247, v247\n\t" \
    "v_exp_f32 v248, v248\n\t" \
    "v_exp_f32 v249, v249\n\t" \
    "v_exp_f32 v250, v250\n\t" \
    "v_exp_f32 v251, v251\n\t" \
    "v_smfmac_f32_32x32x32_bf16 v[48:63], v[208:211], v[240:247], v8\n\t" \
    "v_exp_f32 v252, v252\n\t" \
    "v_exp_f32 v253, v253\n\t" \
    "v_exp_f32 v254, v254\n\t" \
    "v_exp_f32 v255, v255\n\t" \
    "v_exp_f32 v224, v224\n\t" \
    "v_exp_f32 v225, v225\n\t" \
    "v_smfmac_f32_32x32x32_bf16 v[48:63], v[212:215], v[248:255], v8\n\t" \
    "s_mov_b64 exec, s[44:45]\n\t" \
    "global_load_dwordx4 v[208:211], v10, s[40:41] offset:3072 sc1\n\t" \
    "global_load_dwordx4 v[212:215], v10, s[40:41] offset:3328 sc1\n\t" \
    "s_mov_b64 exec, -1\n\t" \
    "v_exp_f32 v226, v226\n\t" \
    "v_exp_f32 v227, v227\n\t" \
    "v_exp_f32 v228, v228\n\t" \
    "v_mfma_f32_32x32x16_bf16 v[240:255], v[188:191], v[28:31], 0\n\t" \
    "global_load_dwordx4 v[184:187], v11, s[40:41] offset:2048 sc1\n\t" \
    "v_exp_f32 v229, v229\n\t" \
    "v_exp_f32 v230, v230\n\t" \
    "v_exp_f32 v231, v231\n\t" \
    "v_exp_f32 v232, v232\n\t" \
    "v_exp_f32 v233, v233\n\t" \
    "v_exp_f32 v234, v234\n\t" \
    "v_exp_f32 v235, v235\n\t" \
    "v_smfmac_f32_32x32x32_bf16 v[64:79], v[216:219], v[224:231], v8\n\t" \
    "buffer_load_dwordx4 v12, s[76:79], s46 offen lds\n\t" \
    "v_exp_f32 v236, v236\n\t" \
    "v_exp_f32 v237, v237\n\t" \
    "v_exp_f32 v238, v238\n\t" \
    "v_exp_f32 v239, v239\n\t" \
    "v_exp_f32 v240, v240\n\t" \
    "v_exp_f32 v241, v241\n\t" \
    "v_smfmac_f32_32x32x32_bf16 v[64:79], v[220:223], v[232:239], v8\n\t" \
    "s_addk_i32 s40, 0x1000\n\t" \
    "s_bitset0_b32 s40, s48\n\t" \
    "v_exp_f32 v242, v242\n\t" \
    "v_exp_f32 v243, v243\n\t" \
    "v_exp_f32 v244, v244\n\t" \
    "s_waitcnt vmcnt(8)\n\t" \
    "v_mfma_f32_32x32x16_bf16 v[224:239], v[176:179], v[24:27], 0\n\t" \
    "v_exp_f32 v245, v245\n\t" \
    "v_exp_f32 v246, v246\n\t" \
    "v_exp_f32 v247, v247\n\t" \
    "v_exp_f32 v248, v248\n\t" \
    "v_exp_f32 v249, v249\n\t" \
    "v_exp_f32 v250, v250\n\t" \
    "v_exp_f32 v251, v251\n\t" \
    "v_smfmac_f32_32x32x32_bf16 v[48:63], v[216:219], v[240:247], v8\n\t" \
    "s_add_u32 s46, s46, s52\n\t" \
    "s_add_u32 m0, m0, s53\n\t" \
    "v_exp_f32 v252, v252\n\t" \
    "v_exp_f32 v253, v253\n\t" \
    "v_exp_f32 v254, v254\n\t" \
    "v_exp_f32 v255, v255\n\t" \
    "v_exp_f32 v224, v224\n\t" \
    "v_exp_f32 v225, v225\n\t" \
    "v_smfmac_f32_32x32x32_bf16 v[48:63], v[220:223], v[248:255], v8\n\t" \
    "s_mov_b64 exec, s[44:45]\n\t" \
    "global_load_dwordx4 v[216:219], v10, s[40:41] offset:1024 sc1\n\t" \
    "global_load_dwordx4 v[220:223], v10, s[40:41] offset:1280 sc1\n\t" \
    "s_mov_b64 exec, -1\n\t" \
    "v_exp_f32 v226, v226\n\t" \
    "v_exp_f32 v227, v227\n\t" \
    "v_exp_f32 v228, v228\n\t" \
    "v_mfma_f32_32x32x16_bf16 v[240:255], v[176:179], v[28:31], 0\n\t" \
    "global_load_dwordx4 v[188:191], v11, s[40:41] offset:0 sc1\n\t" \
    "v_exp_f32 v229, v229\n\t" \
    "v_exp_f32 v230, v230\n\t" \
    "v_exp_f32 v231, v231\n\t" \
    "v_exp_f32 v232, v232\n\t" \
    "v_exp_f32 v233, v233\n\t" \
    "v_exp_f32 v234, v234\n\t" \
    "v_exp_f32 v235, v235\n\t" \
    "v_smfmac_f32_32x32x32_bf16 v[64:79], v[192:195], v[224:231], v8\n\t" \
    "v_exp_f32 v236, v236\n\t" \
    "v_exp_f32 v237, v237\n\t" \
    "v_exp_f32 v238, v238\n\t" \
    "v_exp_f32 v239, v239\n\t" \
    "v_exp_f32 v240, v240\n\t" \
    "v_exp_f32 v241, v241\n\t" \
    "v_smfmac_f32_32x32x32_bf16 v[64:79], v[196:199], v[232:239], v8\n\t" \
    "s_add_i32 s47, s47, -1\n\t" \
    "v_exp_f32 v242, v242\n\t" \
    "v_exp_f32 v243, v243\n\t" \
    "v_exp_f32 v244, v244\n\t" \
    "s_waitcnt vmcnt(7)\n\t" \
    "v_mfma_f32_32x32x16_bf16 v[224:239], v[180:183], v[24:27], 0\n\t" \
    "v_exp_f32 v245, v245\n\t" \
    "v_exp_f32 v246, v246\n\t" \
    "v_exp_f32 v247, v247\n\t" \
    "v_exp_f32 v248, v248\n\t" \
    "v_exp_f32 v249, v249\n\t" \
    "v_exp_f32 v250, v250\n\t" \
    "v_exp_f32 v251, v251\n\t" \
    "v_smfmac_f32_32x32x32_bf16 v[48:63], v[192:195], v[240:247], v8\n\t" \
    "s_cmp_eq_u32 s47, 0\n\t" \
    "v_exp_f32 v252, v252\n\t" \
    "v_exp_f32 v253, v253\n\t" \
    "v_exp_f32 v254, v254\n\t" \
    "v_exp_f32 v255, v255\n\t" \
    "s_cbranch_scc0 1b\n\t" \
      \
    "v_cmp_ne_u64_e64 s[42:43], s[36:37], v[20:21]\n\t" \
    "s_cmp_lg_u64 s[42:43], 0\n\t" \
    "s_cbranch_scc1 220f\n\t" \
    "222:\n\t" \
    "v_exp_f32 v224, v224\n\t" \
    "v_exp_f32 v225, v225\n\t" \
    "v_smfmac_f32_32x32x32_bf16 v[48:63], v[196:199], v[248:255], v8\n\t" \
    "s_mov_b64 exec, s[44:45]\n\t" \
    "global_load_dwordx4 v[192:195], v10, s[40:41] offset:3072 sc1\n\t" \
    "global_load_dwordx4 v[196:199], v10, s[40:41] offset:3328 sc1\n\t" \
    "s_mov_b64 exec, -1\n\t" \
    "v_exp_f32 v226, v226\n\t" \
    "v_exp_f32 v227, v227\n\t" \
    "v_exp_f32 v228, v228\n\t" \
    "v_mfma_f32_32x32x16_bf16 v[240:255], v[180:183], v[28:31], 0\n\t" \
    "global_load_dwordx4 v[176:179], v11, s[40:41] offset:2048 sc1\n\t" \
    "ds_read_b64_tr_b16 v[80:81], v0 offset:0\n\t" \
    "ds_read_b64_tr_b16 v[82:83], v0 offset:1024\n\t" \
    "v_exp_f32 v229, v229\n\t" \
    "v_exp_f32 v230, v230\n\t" \
    "v_exp_f32 v231, v231\n\t" \
    "v_exp_f32 v232, v232\n\t" \
    "v_exp_f32 v233, v233\n\t" \
    "v_exp_f32 v234, v234\n\t" \
    "v_exp_f32 v235, v235\n\t" \
    "v_smfmac_f32_32x32x32_bf16 v[64:79], v[200:203], v[224:231], v8\n\t" \
    "ds_read_b64_tr_b16 v[84:85], v1 offset:0\n\t" \
    "ds_read_b64_tr_b16 v[86:87], v1 offset:1024\n\t" \
    "ds_read_b64_tr_b16 v[88:89], v2 offset:0\n\t" \
    "buffer_load_dwordx4 v12, s[76:79], s46 offen lds\n\t" \
    "v_exp_f32 v236, v236\n\t" \
    "v_exp_f32 v237, v237\n\t" \
    "v_exp_f32 v238, v238\n\t" \
    "v_exp_f32 v239, v239\n\t" \
    "v_exp_f32 v240, v240\n\t" \
    "v_exp_f32 v241, v241\n\t" \
    "v_smfmac_f32_32x32x32_bf16 v[64:79], v[204:207], v[232:239], v8\n\t" \
    "ds_read_b64_tr_b16 v[90:91], v2 offset:1024\n\t" \
    "ds_read_b64_tr_b16 v[92:93], v3 offset:0\n\t" \
    "v_exp_f32 v242, v242\n\t" \
    "v_exp_f32 v243, v243\n\t" \
    "v_exp_f32 v244, v244\n\t" \
    "s_waitcnt vmcnt(8)\n\t" \
    "v_mfma_f32_32x32x16_bf16 v[224:239], v[184:187], v[24:27], 0\n\t" \
    "ds_read_b64_tr_b16 v[94:95], v3 offset:1024\n\t" \
    "ds_read_b64_tr_b16 v[96:97], v0 offset:4096\n\t" \
    "ds_read_b64_tr_b16 v[98:99], v0 offset:5120\n\t" \
    "v_exp_f32 v245, v245\n\t" \
    "v_exp_f32 v246, v246\n\t" \
    "v_exp_f32 v247, v247\n\t" \
    "v_exp_f32 v248, v248\n\t" \
    "v_exp_f32 v249, v249\n\t" \
    "v_exp_f32 v250, v250\n\t" \
    "v_exp_f32 v251, v251\n\t" \
    "v_smfmac_f32_32x32x32_bf16 v[48:63], v[200:203], v[240:247], v8\n\t" \
    "ds_read_b64_tr_b16 v[100:101], v1 offset:4096\n\t" \
    "ds_read_b64_tr_b16 v[102:103], v1 offset:5120\n\t" \
    "s_add_u32 s46, s46, s52\n\t" \
    "s_add_u32 m0, m0, s53\n\t" \
    "v_exp_f32 v252, v252\n\t" \
    "v_exp_f32 v253, v253\n\t" \
    "v_exp_f32 v254, v254\n\t" \
    "v_exp_f32 v255, v255\n\t" \
    "v_exp_f32 v224, v224\n\t" \
    "v_exp_f32 v225, v225\n\t" \
    "v_smfmac_f32_32x32x32_bf16 v[48:63], v[204:207], v[248:255], v8\n\t" \
    "buffer_load_dwordx4 v[200:203], v9, s[56:59], s49 offen offset:1024 sc1\n\t" \
    "buffer_load_dwordx4 v[204:207], v9, s[56:59], s49 offen offset:2048 sc1\n\t" \
    "v_exp_f32 v226, v226\n\t" \
    "v_exp_f32 v227, v227\n\t" \
    "v_exp_f32 v228, v228\n\t" \
    "v_mfma_f32_32x32x16_bf16 v[240:255], v[184:187], v[28:31], 0\n\t" \
    "buffer_load_dwordx4 v[180:183], v9, s[56:59], s49 offen offset:0 sc1\n\t" \
    "ds_read_b64_tr_b16 v[104:105], v2 offset:4096\n\t" \
    "ds_read_b64_tr_b16 v[106:107], v2 offset:5120\n\t" \
    "v_exp_f32 v229, v229\n\t" \
    "v_exp_f32 v230, v230\n\t" \
    "v_exp_f32 v231, v231\n\t" \
    "v_exp_f32 v232, v232\n\t" \
    "v_exp_f32 v233, v233\n\t" \
    "v_exp_f32 v234, v234\n\t" \
    "v_exp_f32 v235, v235\n\t" \
    "v_smfmac_f32_32x32x32_bf16 v[64:79], v[208:211], v[224:231], v8\n\t" \
    "ds_read_b64_tr_b16 v[108:109], v3 offset:4096\n\t" \
    "ds_read_b64_tr_b16 v[110:111], v3 offset:5120\n\t" \
    "ds_read_b64_tr_b16 v[112:113], v0 offset:8192\n\t" \
    "buffer_load_dwordx4 v12, s[76:79], s46 offen lds\n\t" \
    "v_exp_f32 v236, v236\n\t" \
    "v_exp_f32 v237, v237\n\t" \
    "v_exp_f32 v238, v238\n\t" \
    "v_exp_f32 v239, v239\n\t" \
    "v_exp_f32 v240, v240\n\t" \
    "v_exp_f32 v241, v241\n\t" \
    "v_smfmac_f32_32x32x32_bf16 v[64:79], v[212:215], v[232:239], v8\n\t" \
    "ds_read_b64_tr_b16 v[114:115], v0 offset:9216\n\t" \
    "ds_read_b64_tr_b16 v[116:117], v1 offset:8192\n\t" \
    "v_exp_f32 v242, v242\n\t" \
    "v_exp_f32 v243, v243\n\t" \
    "v_exp_f32 v244, v244\n\t" \
    "s_waitcnt vmcnt(8)\n\t" \
    "v_mfma_f32_32x32x16_bf16 v[224:239], v[188:191], v[24:27], 0\n\t" \
    "ds_read_b64_tr_b16 v[118:119], v1 offset:9216\n\t" \
    "ds_read_b64_tr_b16 v[120:121], v2 offset:8192\n\t" \
    "ds_read_b64_tr_b16 v[122:123], v2 offset:9216\n\t" \
    "v_exp_f32 v245, v245\n\t" \
    "v_exp_f32 v246, v246\n\t" \
    "v_exp_f32 v247, v247\n\t" \
    "v_exp_f32 v248, v248\n\t" \
    "v_exp_f32 v249, v249\n\t" \
    "v_exp_f32 v250, v250\n\t" \
    "v_exp_f32 v251, v251\n\t" \
    "v_smfmac_f32_32x32x32_bf16 v[48:63], v[208:211], v[240:247], v8\n\t" \
    "ds_read_b64_tr_b16 v[124:125], v3 offset:8192\n\t" \
    "ds_read_b64_tr_b16 v[126:127], v3 offset:9216\n\t" \
    "s_add_u32 s46, s46, s52\n\t" \
    "s_add_u32 m0, m0, s53\n\t" \
    "v_exp_f32 v252, v252\n\t" \
    "v_exp_f32 v253, v253\n\t" \
    "v_exp_f32 v254, v254\n\t" \
    "v_exp_f32 v255, v255\n\t" \
    "v_exp_f32 v224, v224\n\t" \
    "v_exp_f32 v225, v225\n\t" \
    "v_smfmac_f32_32x32x32_bf16 v[48:63], v[212:215], v[248:255], v8\n\t" \
    "buffer_load_dwordx4 v[208:211], v9, s[56:59], s51 offen offset:0 sc1\n\t" \
    "buffer_load_dwordx4 v[212:215], v9, s[56:59], s51 offen offset:1024 sc1\n\t" \
    "s_setprio 0\n\t" \
    "v_exp_f32 v226, v226\n\t" \
    "v_exp_f32 v227, v227\n\t" \
    "v_exp_f32 v228, v228\n\t" \
    "v_mfma_f32_32x32x16_bf16 v[240:255], v[188:191], v[28:31], 0\n\t" \
    "buffer_load_dwordx4 v[184:187], v9, s[56:59], s49 offen offset:3072 sc1\n\t" \
    "ds_read_b64_tr_b16 v[128:129], v0 offset:12288\n\t" \
    "ds_read_b64_tr_b16 v[130:131], v0 offset:13312\n\t" \
    "v_exp_f32 v229, v229\n\t" \
    "v_exp_f32 v230, v230\n\t" \
    "v_exp_f32 v231, v231\n\t" \
    "v_exp_f32 v232, v232\n\t" \
    "v_exp_f32 v233, v233\n\t" \
    "v_exp_f32 v234, v234\n\t" \
    "v_exp_f32 v235, v235\n\t" \
    "v_smfmac_f32_32x32x32_bf16 v[64:79], v[216:219], v[224:231], v8\n\t" \
    "ds_read_b64_tr_b16 v[132:133], v1 offset:12288\n\t" \
    "ds_read_b64_tr_b16 v[134:135], v1 offset:13312\n\t" \
    "ds_read_b64_tr_b16 v[136:137], v2 offset:12288\n\t" \
    "v_exp_f32 v236, v236\n\t" \
    "v_exp_f32 v237, v237\n\t" \
    "v_exp_f32 v238, v238\n\t" \
    "v_exp_f32 v239, v239\n\t" \
    "v_exp_f32 v240, v240\n\t" \
    "v_exp_f32 v241, v241\n\t" \
    "v_smfmac_f32_32x32x32_bf16 v[64:79], v[220:223], v[232:239], v8\n\t" \
    "ds_read_b64_tr_b16 v[138:139], v2 offset:13312\n\t" \
    "ds_read_b64_tr_b16 v[140:141], v3 offset:12288\n\t" \
    "v_exp_f32 v242, v242\n\t" \
    "v_exp_f32 v243, v243\n\t" \
    "v_exp_f32 v244, v244\n\t" \
    "s_waitcnt vmcnt(8)\n\t" \
    "v_mfma_f32_32x32x16_bf16 v[224:239], v[176:179], v[24:27], 0\n\t" \
    "ds_read_b64_tr_b16 v[142:143], v3 offset:13312\n\t" \
    "ds_read_b64_tr_b16 v[144:145], v0 offset:16384\n\t" \
    "ds_read_b64_tr_b16 v[146:147], v0 offset:17408\n\t" \
    "v_exp_f32 v245, v245\n\t" \
    "v_exp_f32 v246, v246\n\t" \
    "v_exp_f32 v247, v247\n\t" \
    "v_exp_f32 v248, v248\n\t" \
    "v_exp_f32 v249, v249\n\t" \
    "v_exp_f32 v250, v250\n\t" \
    "v_exp_f32 v251, v251\n\t" \
    "v_smfmac_f32_32x32x32_bf16 v[48:63], v[216:219], v[240:247], v8\n\t" \
    "ds_read_b64_tr_b16 v[148:149], v1 offset:16384\n\t" \
    "ds_read_b64_tr_b16 v[150:151], v1 offset:17408\n\t" \
    "v_exp_f32 v252, v252\n\t" \
    "v_exp_f32 v253, v253\n\t" \
    "v_exp_f32 v254, v254\n\t" \
    "v_exp_f32 v255, v255\n\t" \
    "v_exp_f32 v224, v224\n\t" \
    "v_exp_f32 v225, v225\n\t" \
    "v_smfmac_f32_32x32x32_bf16 v[48:63], v[220:223], v[248:255], v8\n\t" \
    "buffer_load_dwordx4 v[216:219], v9, s[56:59], s51 offen offset:3072 sc1\n\t" \
    "v_exp_f32 v226, v226\n\t" \
    "v_exp_f32 v227, v227\n\t" \
    "v_exp_f32 v228, v228\n\t" \
    "v_mfma_f32_32x32x16_bf16 v[240:255], v[176:179], v[28:31], 0\n\t" \
    "buffer_load_dwordx4 v[188:191], v9, s[56:59], s51 offen offset:2048 sc1\n\t" \
    "ds_read_b64_tr_b16 v[152:153], v2 offset:16384\n\t" \
    "ds_read_b64_tr_b16 v[154:155], v2 offset:17408\n\t" \
    "v_exp_f32 v229, v229\n\t" \
    "v_exp_f32 v230, v230\n\t" \
    "v_exp_f32 v231, v231\n\t" \
    "v_exp_f32 v232, v232\n\t" \
    "v_exp_f32 v233, v233\n\t" \
    "v_exp_f32 v234, v234\n\t" \
    "v_exp_f32 v235, v235\n\t" \
    "v_smfmac_f32_32x32x32_bf16 v[64:79], v[192:195], v[224:231], v8\n\t" \
    "ds_read_b64_tr_b16 v[156:157], v3 offset:16384\n\t" \
    "ds_read_b64_tr_b16 v[158:159], v3 offset:17408\n\t" \
    "ds_read_b64_tr_b16 v[160:161], v0 offset:20480\n\t" \
    "v_exp_f32 v236, v236\n\t" \
    "v_exp_f32 v237, v237\n\t" \
    "v_exp_f32 v238, v238\n\t" \
    "v_exp_f32 v239, v239\n\t" \
    "v_exp_f32 v240, v240\n\t" \
    "v_exp_f32 v241, v241\n\t" \
    "v_smfmac_f32_32x32x32_bf16 v[64:79], v[196:199], v[232:239], v8\n\t" \
    "ds_read_b64_tr_b16 v[162:163], v0 offset:21504\n\t" \
    "ds_read_b64_tr_b16 v[164:165], v1 offset:20480\n\t" \
    "v_exp_f32 v242, v242\n\t" \
    "v_exp_f32 v243, v243\n\t" \
    "v_exp_f32 v244, v244\n\t" \
    "ds_read_b64_tr_b16 v[166:167], v1 offset:21504\n\t" \
    "ds_read_b64_tr_b16 v[168:169], v2 offset:20480\n\t" \
    "ds_read_b64_tr_b16 v[170:171], v2 offset:21504\n\t" \
    "v_exp_f32 v245, v245\n\t" \
    "v_exp_f32 v246, v246\n\t" \
    "v_exp_f32 v247, v247\n\t" \
    "v_exp_f32 v248, v248\n\t" \
    "v_exp_f32 v249, v249\n\t" \
    "v_smfmac_f32_32x32x32_bf16 v[48:63], v[192:195], v[240:247], v8\n\t" \
    "ds_read_b64_tr_b16 v[172:173], v3 offset:20480\n\t" \
    "ds_read_b64_tr_b16 v[174:175], v3 offset:21504\n\t" \
      \
    "v_exp_f32 v250, v250\n\t" \
    "v_exp_f32 v251, v251\n\t" \
    "ds_write_b128 v4, v[64:67]\n\t" \
    "v_exp_f32 v252, v252\n\t" \
    "ds_write_b128 v4, v[68:71] offset:1024\n\t" \
    "v_exp_f32 v253, v253\n\t" \
    "ds_write_b32 v4, v72 offset:2048\n\t" \
    "v_exp_f32 v254, v254\n\t" \
    "ds_write_b32 v15, v15\n\t" \
    "v_exp_f32 v255, v255\n\t" \
    "s_nop 1\n\t" \
    "v_smfmac_f32_32x32x32_bf16 v[48:63], v[196:199], v[248:255], v8\n\t" \
    "s_branch 9f\n\t" \
      \
    "220:\n\t" \
    "s_mov_b32 s50, 0x10000\n\t" \
    "221:\n\t" \
    "s_sleep 2\n\t" \
    "global_load_dwordx2 v[20:21], v5, s[66:67] sc1\n\t" \
    "s_add_i32 s50, s50, -1\n\t" \
    "s_cmp_eq_u32 s50, 0\n\t" \
    "s_cbranch_scc1 222b\n\t" \
    "s_waitcnt vmcnt(0)\n\t" \
    "v_cmp_ne_u64_e64 s[42:43], s[36:37], v[20:21]\n\t" \
    "s_cmp_lg_u64 s[42:43], 0\n\t" \
    "s_cbranch_scc1 221b\n\t" \
    "s_branch 222b\n\t" \
    "3:\n\t" \
    "s_setprio 1\n\t" \
    "s_mov_b64 vcc, -1\n\t" \
    "s_branch 4b\n\t" \
      \
    "5:\n\t" \
    "s_mov_b64 vcc, 0\n\t" \
    "s_cmp_eq_u32 s47, 1\n\t" \
    "s_cbranch_scc1 7f\n\t" \
    "s_mov_b64 exec, 1\n\t" \
    "v_mov_b32 v16, 133632\n\t" \
    "v_mov_b32 v17, 1\n\t" \
    "ds_add_rtn_u32 v17, v16, v17\n\t" \
    "v_mov_b64 v[18:19], s[36:37]\n\t" \
    "s_waitcnt lgkmcnt(0)\n\t" \
    "v_readfirstlane_b32 s50, v17\n\t" \
    "s_cmp_lg_u32 s50, 7\n\t" \
    "s_cbranch_scc1 31f\n\t" \
    "global_store_dwordx2 v9, v[18:19], s[64:65] offset:0 sc1\n\t" \
    "31:\n\t" \
    "s_mov_b64 exec, -1\n\t" \
    "s_setprio 2\n\t" \
    "s_branch 6b\n\t" \
    "7:\n\t" \
    "global_load_dwordx2 v[20:21], v5, s[66:67] sc1\n\t" \
    "s_branch 6b\n\t" \
      \
    "9:\n\t" \
    "v_add_u32 v4, s89, v9\n\t" \
    "v_xor_b32 v15, 16, v15\n\t" \
    "s_mov_b32 s50, 0x100000\n\t" \
    "12:\n\t" \
    "ds_read_b32 v7, v15\n\t" \
    "ds_read_b128 v[16:19], v4\n\t" \
    "ds_read_b128 v[20:23], v4 offset:1024\n\t" \
    "ds_read_b32 v13, v4 offset:2048\n\t" \
    "s_add_i32 s50, s50, -1\n\t" \
    "s_waitcnt lgkmcnt(3)\n\t" \
    "v_readfirstlane_b32 s51, v7\n\t" \
    "s_cmp_eq_u32 s51, 0\n\t" \
    "s_cselect_b32 s51, s50, 0\n\t" \
    "s_cmp_lg_u32 s51, 0\n\t" \
    "s_cbranch_scc1 12b\n\t" \
    "s_waitcnt vmcnt(0) lgkmcnt(0)\n\t" \
    "v_add_f32 v56, v56, v13\n\t" \
    "v_pk_add_f32 v[48:49], v[48:49], v[16:17]\n\t" \
    "v_pk_add_f32 v[50:51], v[50:51], v[18:19]\n\t" \
    "v_pk_add_f32 v[52:53], v[52:53], v[20:21]\n\t" \
    "v_pk_add_f32 v[54:55], v[54:55], v[22:23]\n\t" \
      \
    "v_pk_mul_f32 v[20:21], v[48:49], 0\n\t" \
    "v_pk_fma_f32 v[20:21], v[50:51], 0, v[20:21]\n\t" \
    "v_pk_fma_f32 v[20:21], v[52:53], 0, v[20:21]\n\t" \
    "v_pk_fma_f32 v[20:21], v[54:55], 0, v[20:21]\n\t" \
    "v_add_f32 v20, v20, v21\n\t" \
    "v_add_f32 v20, v20, v56\n\t" \
    "v_subrev_u32 v20, 0x2b800000, v20\n\t" \
    "v_cmp_lt_u32_e32 vcc, 0x41000000, v20\n\t" \
    "v_rcp_f32 v56, v56\n\t" \
    "s_nop 0\n\t" \
    "v_mul_f32 v56, v14, v56\n\t" \
    "v_pk_mul_f32 v[48:49], v[48:49], v[56:57] op_sel_hi:[1,0]\n\t" \
    "v_pk_mul_f32 v[50:51], v[50:51], v[56:57] op_sel_hi:[1,0]\n\t" \
    "v_pk_mul_f32 v[52:53], v[52:53], v[56:57] op_sel_hi:[1,0]\n\t" \
    "v_pk_mul_f32 v[54:55], v[54:55], v[56:57] op_sel_hi:[1,0]\n\t" \
    "v_cvt_pk_bf16_f32 v16, v48, v49\n\t" \
    "v_cvt_pk_bf16_f32 v17, v50, v51\n\t" \
    "v_cvt_pk_bf16_f32 v18, v52, v53\n\t" \
    "v_cvt_pk_bf16_f32 v19, v54, v55\n\t" \
    "s_mov_b64 s[38:39], vcc\n\t" \
    ""
#define PART2R_ASM \
    "s_add_u32 s49, s61, 0x200000\n\t" \
    "s_add_u32 s51, s61, 0x201000\n\t" \
    "buffer_load_dwordx4 v[180:183], v9, s[56:59], s49 offen offset:0 sc1\n\t" \
    "buffer_load_dwordx4 v[200:203], v9, s[56:59], s49 offen offset:1024 sc1\n\t" \
    "buffer_load_dwordx4 v[204:207], v9, s[56:59], s49 offen offset:2048 sc1\n\t" \
    "buffer_load_dwordx4 v[184:187], v9, s[56:59], s49 offen offset:3072 sc1\n\t" \
    "buffer_load_dwordx4 v[208:211], v9, s[56:59], s51 offen offset:0 sc1\n\t" \
    "buffer_load_dwordx4 v[212:215], v9, s[56:59], s51 offen offset:1024 sc1\n\t" \
    "buffer_load_dwordx4 v[188:191], v9, s[56:59], s51 offen offset:2048 sc1\n\t" \
    "buffer_load_dwordx4 v[216:219], v9, s[56:59], s51 offen offset:3072 sc1\n\t" \
    "ds_read_b64_tr_b16 v[80:81], v0 offset:0\n\t" \
    "ds_read_b64_tr_b16 v[82:83], v0 offset:1024\n\t" \
    "ds_read_b64_tr_b16 v[84:85], v1 offset:0\n\t" \
    "ds_read_b64_tr_b16 v[86:87], v1 offset:1024\n\t" \
    "ds_read_b64_tr_b16 v[88:89], v2 offset:0\n\t" \
    "ds_read_b64_tr_b16 v[90:91], v2 offset:1024\n\t" \
    "ds_read_b64_tr_b16 v[92:93], v3 offset:0\n\t" \
    "ds_read_b64_tr_b16 v[94:95], v3 offset:1024\n\t" \
    "ds_read_b64_tr_b16 v[96:97], v0 offset:4096\n\t" \
    "ds_read_b64_tr_b16 v[98:99], v0 offset:5120\n\t" \
    "ds_read_b64_tr_b16 v[100:101], v1 offset:4096\n\t" \
    "ds_read_b64_tr_b16 v[102:103], v1 offset:5120\n\t" \
    "ds_read_b64_tr_b16 v[104:105], v2 offset:4096\n\t" \
    "ds_read_b64_tr_b16 v[106:107], v2 offset:5120\n\t" \
    "ds_read_b64_tr_b16 v[108:109], v3 offset:4096\n\t" \
    "ds_read_b64_tr_b16 v[110:111], v3 offset:5120\n\t" \
    "ds_read_b64_tr_b16 v[112:113], v0 offset:8192\n\t" \
    "ds_read_b64_tr_b16 v[114:115], v0 offset:9216\n\t" \
    "ds_read_b64_tr_b16 v[116:117], v1 offset:8192\n\t" \
    "ds_read_b64_tr_b16 v[118:119], v1 offset:9216\n\t" \
    "ds_read_b64_tr_b16 v[120:121], v2 offset:8192\n\t" \
    "ds_read_b64_tr_b16 v[122:123], v2 offset:9216\n\t" \
    "ds_read_b64_tr_b16 v[124:125], v3 offset:8192\n\t" \
    "ds_read_b64_tr_b16 v[126:127], v3 offset:9216\n\t" \
    "ds_read_b64_tr_b16 v[128:129], v0 offset:12288\n\t" \
    "ds_read_b64_tr_b16 v[130:131], v0 offset:13312\n\t" \
    "ds_read_b64_tr_b16 v[132:133], v1 offset:12288\n\t" \
    "ds_read_b64_tr_b16 v[134:135], v1 offset:13312\n\t" \
    "ds_read_b64_tr_b16 v[136:137], v2 offset:12288\n\t" \
    "ds_read_b64_tr_b16 v[138:139], v2 offset:13312\n\t" \
    "ds_read_b64_tr_b16 v[140:141], v3 offset:12288\n\t" \
    "ds_read_b64_tr_b16 v[142:143], v3 offset:13312\n\t" \
    "ds_read_b64_tr_b16 v[144:145], v0 offset:16384\n\t" \
    "ds_read_b64_tr_b16 v[146:147], v0 offset:17408\n\t" \
    "ds_read_b64_tr_b16 v[148:149], v1 offset:16384\n\t" \
    "ds_read_b64_tr_b16 v[150:151], v1 offset:17408\n\t" \
    "ds_read_b64_tr_b16 v[152:153], v2 offset:16384\n\t" \
    "ds_read_b64_tr_b16 v[154:155], v2 offset:17408\n\t" \
    "ds_read_b64_tr_b16 v[156:157], v3 offset:16384\n\t" \
    "ds_read_b64_tr_b16 v[158:159], v3 offset:17408\n\t" \
    "ds_read_b64_tr_b16 v[160:161], v0 offset:20480\n\t" \
    "ds_read_b64_tr_b16 v[162:163], v0 offset:21504\n\t" \
    "ds_read_b64_tr_b16 v[164:165], v1 offset:20480\n\t" \
    "ds_read_b64_tr_b16 v[166:167], v1 offset:21504\n\t" \
    "ds_read_b64_tr_b16 v[168:169], v2 offset:20480\n\t" \
    "ds_read_b64_tr_b16 v[170:171], v2 offset:21504\n\t" \
    "ds_read_b64_tr_b16 v[172:173], v3 offset:20480\n\t" \
    "ds_read_b64_tr_b16 v[174:175], v3 offset:21504\n\t" \
    "v_rcp_f32 v56, v56\n\t" \
    "s_nop 0\n\t" \
    "v_mul_f32 v56, v14, v56\n\t" \
    "v_pk_mul_f32 v[48:49], v[48:49], v[56:57] op_sel_hi:[1,0]\n\t" \
    "v_pk_mul_f32 v[50:51], v[50:51], v[56:57] op_sel_hi:[1,0]\n\t" \
    "v_pk_mul_f32 v[52:53], v[52:53], v[56:57] op_sel_hi:[1,0]\n\t" \
    "v_pk_mul_f32 v[54:55], v[54:55], v[56:57] op_sel_hi:[1,0]\n\t" \
    "v_cvt_pk_bf16_f32 v16, v48, v49\n\t" \
    "v_cvt_pk_bf16_f32 v17, v50, v51\n\t" \
    "v_cvt_pk_bf16_f32 v18, v52, v53\n\t" \
    "v_cvt_pk_bf16_f32 v19, v54, v55\n\t" \
    "s_waitcnt vmcnt(0) lgkmcnt(0)\n\t" \
    ""
#define PART3_ASM \
      \
    "v_min_u32 v16, -2, v16\n\t" \
    "v_min_u32 v17, -2, v17\n\t" \
    "v_min_u32 v18, -2, v18\n\t" \
    "v_min_u32 v19, -2, v19\n\t" \
    "buffer_store_dwordx4 v[16:19], v6, s[56:59], s60 offen sc1\n\t" \
    "s_mov_b32 s49, s61\n\t" \
    "s_add_u32 s51, s61, 0x1000\n\t" \
    "s_mov_b32 s50, 0x10000\n\t" \
      \
    "s_barrier\n\t" \
    "buffer_load_dwordx4 v[48:51], v9, s[56:59], s49 offen offset:0 sc1\n\t" \
    "buffer_load_dwordx4 v[52:55], v9, s[56:59], s49 offen offset:2048 sc1\n\t" \
    "buffer_load_dwordx4 v[56:59], v9, s[56:59], s51 offen offset:0 sc1\n\t" \
    "buffer_load_dwordx4 v[60:63], v9, s[56:59], s51 offen offset:2048 sc1\n\t" \
    "s_setprio 3\n\t" \
    "v_mfma_f32_16x16x32_bf16 v[32:35], v[80:83], v[180:183], 0\n\t" \
    "ds_read_b64_tr_b16 v[224:225], v0 offset:24576\n\t" \
    "ds_read_b64_tr_b16 v[226:227], v0 offset:25600\n\t" \
    "v_mfma_f32_16x16x32_bf16 v[36:39], v[84:87], v[180:183], 0\n\t" \
    "ds_read_b64_tr_b16 v[228:229], v1 offset:24576\n\t" \
    "ds_read_b64_tr_b16 v[230:231], v1 offset:25600\n\t" \
    "v_mfma_f32_16x16x32_bf16 v[40:43], v[88:91], v[180:183], 0\n\t" \
    "ds_read_b64_tr_b16 v[232:233], v2 offset:24576\n\t" \
    "ds_read_b64_tr_b16 v[234:235], v2 offset:25600\n\t" \
    "v_mfma_f32_16x16x32_bf16 v[44:47], v[92:95], v[180:183], 0\n\t" \
    "ds_read_b64_tr_b16 v[236:237], v3 offset:24576\n\t" \
    "ds_read_b64_tr_b16 v[238:239], v3 offset:25600\n\t" \
    "v_mfma_f32_16x16x32_bf16 v[32:35], v[112:115], v[204:207], v[32:35]\n\t" \
    "ds_read_b64_tr_b16 v[240:241], v0 offset:28672\n\t" \
    "ds_read_b64_tr_b16 v[242:243], v0 offset:29696\n\t" \
    "v_mfma_f32_16x16x32_bf16 v[36:39], v[116:119], v[204:207], v[36:39]\n\t" \
    "ds_read_b64_tr_b16 v[244:245], v1 offset:28672\n\t" \
    "ds_read_b64_tr_b16 v[246:247], v1 offset:29696\n\t" \
    "v_mfma_f32_16x16x32_bf16 v[40:43], v[120:123], v[204:207], v[40:43]\n\t" \
    "ds_read_b64_tr_b16 v[248:249], v2 offset:28672\n\t" \
    "ds_read_b64_tr_b16 v[250:251], v2 offset:29696\n\t" \
    "v_mfma_f32_16x16x32_bf16 v[44:47], v[124:127], v[204:207], v[44:47]\n\t" \
    "ds_read_b64_tr_b16 v[252:253], v3 offset:28672\n\t" \
    "ds_read_b64_tr_b16 v[254:255], v3 offset:29696\n\t" \
    "v_add_u32 v0, 0xffff0000, v0\n\t" \
    "v_add_u32 v1, 0xffff0000, v1\n\t" \
    "v_add_u32 v2, 0xffff0000, v2\n\t" \
    "v_add_u32 v3, 0xffff0000, v3\n\t" \
    "v_mfma_f32_16x16x32_bf16 v[32:35], v[144:147], v[208:211], v[32:35]\n\t" \
    "ds_read_b64_tr_b16 v[80:81], v0 offset:0\n\t" \
    "ds_read_b64_tr_b16 v[82:83], v0 offset:1024\n\t" \
    "v_mfma_f32_16x16x32_bf16 v[36:39], v[148:151], v[208:211], v[36:39]\n\t" \
    "ds_read_b64_tr_b16 v[84:85], v1 offset:0\n\t" \
    "ds_read_b64_tr_b16 v[86:87], v1 offset:1024\n\t" \
    "v_mfma_f32_16x16x32_bf16 v[40:43], v[152:155], v[208:211], v[40:43]\n\t" \
    "ds_read_b64_tr_b16 v[88:89], v2 offset:0\n\t" \
    "ds_read_b64_tr_b16 v[90:91], v2 offset:1024\n\t" \
    "v_mfma_f32_16x16x32_bf16 v[44:47], v[156:159], v[208:211], v[44:47]\n\t" \
    "ds_read_b64_tr_b16 v[92:93], v3 offset:0\n\t" \
    "ds_read_b64_tr_b16 v[94:95], v3 offset:1024\n\t" \
    "v_mfma_f32_16x16x32_bf16 v[32:35], v[96:99], v[200:203], v[32:35]\n\t" \
    "ds_read_b64_tr_b16 v[112:113], v0 offset:8192\n\t" \
    "ds_read_b64_tr_b16 v[114:115], v0 offset:9216\n\t" \
    "v_mfma_f32_16x16x32_bf16 v[36:39], v[100:103], v[200:203], v[36:39]\n\t" \
    "ds_read_b64_tr_b16 v[116:117], v1 offset:8192\n\t" \
    "ds_read_b64_tr_b16 v[118:119], v1 offset:9216\n\t" \
    "v_mfma_f32_16x16x32_bf16 v[40:43], v[104:107], v[200:203], v[40:43]\n\t" \
    "ds_read_b64_tr_b16 v[120:121], v2 offset:8192\n\t" \
    "ds_read_b64_tr_b16 v[122:123], v2 offset:9216\n\t" \
    "v_mfma_f32_16x16x32_bf16 v[44:47], v[108:111], v[200:203], v[44:47]\n\t" \
    "ds_read_b64_tr_b16 v[124:125], v3 offset:8192\n\t" \
    "ds_read_b64_tr_b16 v[126:127], v3 offset:9216\n\t" \
    "s_setprio 2\n\t" \
    "s_waitcnt lgkmcnt(14)\n\t" \
    "v_mfma_f32_16x16x32_bf16 v[32:35], v[224:227], v[188:191], v[32:35]\n\t" \
    "ds_read_b64_tr_b16 v[144:145], v0 offset:16384\n\t" \
    "v_lshrrev_b32 v12, 4, v9\n\t" \
    "v_mfma_f32_16x16x32_bf16 v[36:39], v[228:231], v[188:191], v[36:39]\n\t" \
    "ds_read_b64_tr_b16 v[146:147], v0 offset:17408\n\t" \
    "v_xor_b32 v12, v12, v9\n\t" \
    "v_mfma_f32_16x16x32_bf16 v[40:43], v[232:235], v[188:191], v[40:43]\n\t" \
    "ds_read_b64_tr_b16 v[148:149], v1 offset:16384\n\t" \
    "v_and_b32 v12, 0x70, v12\n\t" \
    "v_mfma_f32_16x16x32_bf16 v[44:47], v[236:239], v[188:191], v[44:47]\n\t" \
    "ds_read_b64_tr_b16 v[150:151], v1 offset:17408\n\t" \
    "v_and_b32 v13, 0xf0, v9\n\t" \
    "v_mfma_f32_16x16x32_bf16 v[32:35], v[128:131], v[184:187], v[32:35]\n\t" \
    "ds_read_b64_tr_b16 v[152:153], v2 offset:16384\n\t" \
    "v_lshl_or_b32 v12, v13, 4, v12\n\t" \
    "v_mfma_f32_16x16x32_bf16 v[36:39], v[132:135], v[184:187], v[36:39]\n\t" \
    "ds_read_b64_tr_b16 v[154:155], v2 offset:17408\n\t" \
    "v_add_u32 v4, s89, v12\n\t" \
    "v_mfma_f32_16x16x32_bf16 v[40:43], v[136:139], v[184:187], v[40:43]\n\t" \
    "ds_read_b64_tr_b16 v[156:157], v3 offset:16384\n\t" \
    "v_xor_b32 v5, 64, v4\n\t" \
    "v_mfma_f32_16x16x32_bf16 v[44:47], v[140:143], v[184:187], v[44:47]\n\t" \
    "ds_read_b64_tr_b16 v[158:159], v3 offset:17408\n\t" \
    "v_lshrrev_b32 v14, 7, v9\n\t" \
    "v_mfma_f32_16x16x32_bf16 v[32:35], v[160:163], v[212:215], v[32:35]\n\t" \
    "ds_read_b64_tr_b16 v[224:225], v0 offset:24576\n\t" \
    "v_and_b32 v12, 0x70, v9\n\t" \
    "v_mfma_f32_16x16x32_bf16 v[36:39], v[164:167], v[212:215], v[36:39]\n\t" \
    "ds_read_b64_tr_b16 v[226:227], v0 offset:25600\n\t" \
    "v_lshlrev_b32 v13, 1, v12\n\t" \
    "v_mfma_f32_16x16x32_bf16 v[40:43], v[168:171], v[212:215], v[40:43]\n\t" \
    "ds_read_b64_tr_b16 v[228:229], v1 offset:24576\n\t" \
    "v_mul_u32_u24 v15, 0x110, v14\n\t" \
    "v_mfma_f32_16x16x32_bf16 v[44:47], v[172:175], v[212:215], v[44:47]\n\t" \
    "ds_read_b64_tr_b16 v[230:231], v1 offset:25600\n\t" \
    "v_xor_b32 v13, v13, v15\n\t" \
    "v_mfma_f32_16x16x32_bf16 v[32:35], v[240:243], v[216:219], v[32:35]\n\t" \
    "ds_read_b64_tr_b16 v[232:233], v2 offset:24576\n\t" \
    "v_add_u32 v13, s90, v13\n\t" \
    "v_mfma_f32_16x16x32_bf16 v[36:39], v[244:247], v[216:219], v[36:39]\n\t" \
    "ds_read_b64_tr_b16 v[234:235], v2 offset:25600\n\t" \
    "v_xor_b32 v15, 16, v13\n\t" \
    "v_mfma_f32_16x16x32_bf16 v[40:43], v[248:251], v[216:219], v[40:43]\n\t" \
    "ds_read_b64_tr_b16 v[236:237], v3 offset:24576\n\t" \
    "v_lshl_or_b32 v12, v14, 10, v12\n\t" \
    "v_mfma_f32_16x16x32_bf16 v[44:47], v[252:255], v[216:219], v[44:47]\n\t" \
    "ds_read_b64_tr_b16 v[238:239], v3 offset:25600\n\t" \
      \
    "s_waitcnt vmcnt(2)\n\t" \
    "buffer_load_dwordx4 v[64:67], v9, s[56:59], s49 offen offset:1024 sc1\n\t" \
    "buffer_load_dwordx4 v[68:71], v9, s[56:59], s49 offen offset:3072 sc1\n\t" \
    "buffer_load_dwordx4 v[72:75], v9, s[56:59], s51 offen offset:1024 sc1\n\t" \
    "buffer_load_dwordx4 v[76:79], v9, s[56:59], s51 offen offset:3072 sc1\n\t" \
    "v_max3_u32 v16, v48, v49, v50\n\t" \
    "v_max3_u32 v17, v51, v52, v53\n\t" \
    "v_max3_u32 v16, v16, v54, v55\n\t" \
    "v_max_u32 v16, v16, v17\n\t" \
    "v_cmp_eq_u32_e64 s[42:43], -1, v16\n\t" \
    "s_cmp_lg_u64 s[42:43], 0\n\t" \
    "s_setprio 1\n\t" \
    "s_cbranch_scc1 70f\n\t" \
    "80:\n\t" \
    "s_waitcnt lgkmcnt(14)\n\t" \
    "v_mfma_f32_16x16x32_bf16 v[40:43], v[88:91], v[48:51], v[40:43]\n\t" \
    "ds_read_b64_tr_b16 v[104:105], v2 offset:4096\n\t" \
    "ds_read_b64_tr_b16 v[106:107], v2 offset:5120\n\t" \
    "v_mfma_f32_16x16x32_bf16 v[40:43], v[120:123], v[52:55], v[40:43]\n\t" \
    "ds_read_b64_tr_b16 v[136:137], v2 offset:12288\n\t" \
    "ds_read_b64_tr_b16 v[138:139], v2 offset:13312\n\t" \
    "v_mfma_f32_16x16x32_bf16 v[44:47], v[92:95], v[48:51], v[44:47]\n\t" \
    "ds_read_b64_tr_b16 v[108:109], v3 offset:4096\n\t" \
    "ds_read_b64_tr_b16 v[110:111], v3 offset:5120\n\t" \
    "v_mfma_f32_16x16x32_bf16 v[44:47], v[124:127], v[52:55], v[44:47]\n\t" \
    "ds_read_b64_tr_b16 v[140:141], v3 offset:12288\n\t" \
    "ds_read_b64_tr_b16 v[142:143], v3 offset:13312\n\t" \
    "v_mfma_f32_16x16x32_bf16 v[32:35], v[80:83], v[48:51], v[32:35]\n\t" \
    "ds_read_b64_tr_b16 v[96:97], v0 offset:4096\n\t" \
    "ds_read_b64_tr_b16 v[98:99], v0 offset:5120\n\t" \
    "v_mfma_f32_16x16x32_bf16 v[32:35], v[112:115], v[52:55], v[32:35]\n\t" \
    "ds_read_b64_tr_b16 v[128:129], v0 offset:12288\n\t" \
    "ds_read_b64_tr_b16 v[130:131], v0 offset:13312\n\t" \
    "v_mfma_f32_16x16x32_bf16 v[36:39], v[84:87], v[48:51], v[36:39]\n\t" \
    "ds_read_b64_tr_b16 v[100:101], v1 offset:4096\n\t" \
    "ds_read_b64_tr_b16 v[102:103], v1 offset:5120\n\t" \
    "v_mfma_f32_16x16x32_bf16 v[36:39], v[116:119], v[52:55], v[36:39]\n\t" \
    "ds_read_b64_tr_b16 v[132:133], v1 offset:12288\n\t" \
    "ds_read_b64_tr_b16 v[134:135], v1 offset:13312\n\t" \
    "s_waitcnt vmcnt(4)\n\t" \
    "v_max3_u32 v16, v56, v57, v58\n\t" \
    "v_max3_u32 v17, v59, v60, v61\n\t" \
    "v_max3_u32 v16, v16, v62, v63\n\t" \
    "v_max_u32 v16, v16, v17\n\t" \
    "v_cmp_eq_u32_e64 s[42:43], -1, v16\n\t" \
    "s_cmp_lg_u64 s[42:43], 0\n\t" \
    "s_cbranch_scc1 71f\n\t" \
    "81:\n\t" \
    "s_waitcnt lgkmcnt(14)\n\t" \
    "v_mfma_f32_16x16x32_bf16 v[40:43], v[152:155], v[56:59], v[40:43]\n\t" \
    "ds_read_b64_tr_b16 v[168:169], v2 offset:20480\n\t" \
    "ds_read_b64_tr_b16 v[170:171], v2 offset:21504\n\t" \
    "v_mfma_f32_16x16x32_bf16 v[40:43], v[232:235], v[60:63], v[40:43]\n\t" \
    "ds_read_b64_tr_b16 v[248:249], v2 offset:28672\n\t" \
    "ds_read_b64_tr_b16 v[250:251], v2 offset:29696\n\t" \
    "v_mfma_f32_16x16x32_bf16 v[44:47], v[156:159], v[56:59], v[44:47]\n\t" \
    "ds_read_b64_tr_b16 v[172:173], v3 offset:20480\n\t" \
    "ds_read_b64_tr_b16 v[174:175], v3 offset:21504\n\t" \
    "v_mfma_f32_16x16x32_bf16 v[44:47], v[236:239], v[60:63], v[44:47]\n\t" \
    "ds_read_b64_tr_b16 v[252:253], v3 offset:28672\n\t" \
    "ds_read_b64_tr_b16 v[254:255], v3 offset:29696\n\t" \
    "v_mfma_f32_16x16x32_bf16 v[32:35], v[144:147], v[56:59], v[32:35]\n\t" \
    "ds_read_b64_tr_b16 v[160:161], v0 offset:20480\n\t" \
    "ds_read_b64_tr_b16 v[162:163], v0 offset:21504\n\t" \
    "v_mfma_f32_16x16x32_bf16 v[32:35], v[224:227], v[60:63], v[32:35]\n\t" \
    "ds_read_b64_tr_b16 v[240:241], v0 offset:28672\n\t" \
    "ds_read_b64_tr_b16 v[242:243], v0 offset:29696\n\t" \
    "v_mfma_f32_16x16x32_bf16 v[36:39], v[148:151], v[56:59], v[36:39]\n\t" \
    "ds_read_b64_tr_b16 v[164:165], v1 offset:20480\n\t" \
    "ds_read_b64_tr_b16 v[166:167], v1 offset:21504\n\t" \
    "v_mfma_f32_16x16x32_bf16 v[36:39], v[228:231], v[60:63], v[36:39]\n\t" \
    "ds_read_b64_tr_b16 v[244:245], v1 offset:28672\n\t" \
    "ds_read_b64_tr_b16 v[246:247], v1 offset:29696\n\t" \
    "s_waitcnt vmcnt(2)\n\t" \
    "v_max3_u32 v16, v64, v65, v66\n\t" \
    "v_max3_u32 v17, v67, v68, v69\n\t" \
    "v_max3_u32 v16, v16, v70, v71\n\t" \
    "v_max_u32 v16, v16, v17\n\t" \
    "v_cmp_eq_u32_e64 s[42:43], -1, v16\n\t" \
    "s_cmp_lg_u64 s[42:43], 0\n\t" \
    "s_setprio 0\n\t" \
    "s_cbranch_scc1 72f\n\t" \
    "82:\n\t" \
    "s_waitcnt lgkmcnt(14)\n\t" \
    "v_mfma_f32_16x16x32_bf16 v[40:43], v[104:107], v[64:67], v[40:43]\n\t" \
    "v_mfma_f32_16x16x32_bf16 v[40:43], v[136:139], v[68:71], v[40:43]\n\t" \
    "v_mfma_f32_16x16x32_bf16 v[44:47], v[108:111], v[64:67], v[44:47]\n\t" \
    "v_mfma_f32_16x16x32_bf16 v[44:47], v[140:143], v[68:71], v[44:47]\n\t" \
    "v_mfma_f32_16x16x32_bf16 v[32:35], v[96:99], v[64:67], v[32:35]\n\t" \
    "v_mfma_f32_16x16x32_bf16 v[32:35], v[128:131], v[68:71], v[32:35]\n\t" \
    "v_mfma_f32_16x16x32_bf16 v[36:39], v[100:103], v[64:67], v[36:39]\n\t" \
    "v_mfma_f32_16x16x32_bf16 v[36:39], v[132:135], v[68:71], v[36:39]\n\t" \
    "s_waitcnt vmcnt(0)\n\t" \
    "v_max3_u32 v16, v72, v73, v74\n\t" \
    "v_max3_u32 v17, v75, v76, v77\n\t" \
    "v_max3_u32 v16, v16, v78, v79\n\t" \
    "v_max_u32 v16, v16, v17\n\t" \
    "v_cmp_eq_u32_e64 s[42:43], -1, v16\n\t" \
    "s_cmp_lg_u64 s[42:43], 0\n\t" \
    "s_cbranch_scc1 73f\n\t" \
    "83:\n\t" \
    "s_waitcnt lgkmcnt(0)\n\t" \
    "v_mfma_f32_16x16x32_bf16 v[40:43], v[168:171], v[72:75], v[40:43]\n\t" \
    "v_mfma_f32_16x16x32_bf16 v[40:43], v[248:251], v[76:79], v[40:43]\n\t" \
    "v_mfma_f32_16x16x32_bf16 v[44:47], v[172:175], v[72:75], v[44:47]\n\t" \
    "v_mfma_f32_16x16x32_bf16 v[44:47], v[252:255], v[76:79], v[44:47]\n\t" \
    "v_mfma_f32_16x16x32_bf16 v[32:35], v[160:163], v[72:75], v[32:35]\n\t" \
    "v_mfma_f32_16x16x32_bf16 v[32:35], v[240:243], v[76:79], v[32:35]\n\t" \
    "v_mfma_f32_16x16x32_bf16 v[36:39], v[164:167], v[72:75], v[36:39]\n\t" \
    "v_mfma_f32_16x16x32_bf16 v[36:39], v[244:247], v[76:79], v[36:39]\n\t" \
      \
    "s_nop 3\n\t" \
    "ds_write_b128 v4, v[40:43] offset:128\n\t" \
    "ds_write_b128 v5, v[44:47] offset:128\n\t" \
    "ds_write_b128 v4, v[32:35]\n\t" \
    "s_nop 0\n\t" \
    "ds_write_b128 v5, v[36:39]\n\t" \
    "s_waitcnt lgkmcnt(0)\n\t" \
    "s_barrier\n\t" \
    "ds_read_b128 v[16:19], v13\n\t" \
    "ds_read_b128 v[32:35], v13 offset:16384\n\t" \
    "ds_read_b128 v[20:23], v15\n\t" \
    "ds_read_b128 v[36:39], v15 offset:16384\n\t" \
    "s_waitcnt lgkmcnt(2)\n\t" \
    "v_pk_add_f32 v[32:33], v[16:17], v[32:33]\n\t" \
    "v_pk_add_f32 v[34:35], v[18:19], v[34:35]\n\t" \
    "v_cvt_pk_bf16_f32 v16, v32, v33\n\t" \
    "v_cvt_pk_bf16_f32 v17, v34, v35\n\t" \
    "s_waitcnt lgkmcnt(0)\n\t" \
    "v_pk_add_f32 v[36:37], v[20:21], v[36:37]\n\t" \
    "v_pk_add_f32 v[38:39], v[22:23], v[38:39]\n\t" \
    "v_cvt_pk_bf16_f32 v18, v36, v37\n\t" \
    "v_cvt_pk_bf16_f32 v19, v38, v39\n\t" \
    "buffer_store_dwordx4 v[16:19], v12, s[84:87], s88 offen sc1\n\t" \
    "s_branch 9f\n\t" \
      \
    "70:\n\t" \
    "s_add_i32 s50, s50, -1\n\t" \
    "s_cmp_le_i32 s50, 0\n\t" \
    "s_cbranch_scc1 80b\n\t" \
    "s_waitcnt vmcnt(0)\n\t" \
    "s_sleep 2\n\t" \
    "buffer_load_dwordx4 v[48:51], v9, s[56:59], s49 offen offset:0 sc1\n\t" \
    "buffer_load_dwordx4 v[52:55], v9, s[56:59], s49 offen offset:2048 sc1\n\t" \
    "v_max3_u32 v16, v56, v57, v58\n\t" \
    "v_max3_u32 v17, v59, v60, v61\n\t" \
    "v_max3_u32 v16, v16, v62, v63\n\t" \
    "v_max_u32 v16, v16, v17\n\t" \
    "v_cmp_eq_u32_e64 s[42:43], -1, v16\n\t" \
    "s_cmp_lg_u64 s[42:43], 0\n\t" \
    "s_cbranch_scc0 91f\n\t" \
    "buffer_load_dwordx4 v[56:59], v9, s[56:59], s51 offen offset:0 sc1\n\t" \
    "buffer_load_dwordx4 v[60:63], v9, s[56:59], s51 offen offset:2048 sc1\n\t" \
    "91:\n\t" \
    "v_max3_u32 v16, v64, v65, v66\n\t" \
    "v_max3_u32 v17, v67, v68, v69\n\t" \
    "v_max3_u32 v16, v16, v70, v71\n\t" \
    "v_max_u32 v16, v16, v17\n\t" \
    "v_cmp_eq_u32_e64 s[42:43], -1, v16\n\t" \
    "s_cmp_lg_u64 s[42:43], 0\n\t" \
    "s_cbranch_scc0 92f\n\t" \
    "buffer_load_dwordx4 v[64:67], v9, s[56:59], s49 offen offset:1024 sc1\n\t" \
    "buffer_load_dwordx4 v[68:71], v9, s[56:59], s49 offen offset:3072 sc1\n\t" \
    "92:\n\t" \
    "v_max3_u32 v16, v72, v73, v74\n\t" \
    "v_max3_u32 v17, v75, v76, v77\n\t" \
    "v_max3_u32 v16, v16, v78, v79\n\t" \
    "v_max_u32 v16, v16, v17\n\t" \
    "v_cmp_eq_u32_e64 s[42:43], -1, v16\n\t" \
    "s_cmp_lg_u64 s[42:43], 0\n\t" \
    "s_cbranch_scc0 93f\n\t" \
    "buffer_load_dwordx4 v[72:75], v9, s[56:59], s51 offen offset:1024 sc1\n\t" \
    "buffer_load_dwordx4 v[76:79], v9, s[56:59], s51 offen offset:3072 sc1\n\t" \
    "93:\n\t" \
    "s_waitcnt vmcnt(0)\n\t" \
    "v_max3_u32 v16, v48, v49, v50\n\t" \
    "v_max3_u32 v17, v51, v52, v53\n\t" \
    "v_max3_u32 v16, v16, v54, v55\n\t" \
    "v_max_u32 v16, v16, v17\n\t" \
    "v_cmp_eq_u32_e64 s[42:43], -1, v16\n\t" \
    "s_cmp_lg_u64 s[42:43], 0\n\t" \
    "s_cbranch_scc1 70b\n\t" \
    "s_branch 80b\n\t" \
    "71:\n\t" \
    "s_add_i32 s50, s50, -1\n\t" \
    "s_cmp_le_i32 s50, 0\n\t" \
    "s_cbranch_scc1 81b\n\t" \
    "s_waitcnt vmcnt(0)\n\t" \
    "s_sleep 2\n\t" \
    "buffer_load_dwordx4 v[56:59], v9, s[56:59], s51 offen offset:0 sc1\n\t" \
    "buffer_load_dwordx4 v[60:63], v9, s[56:59], s51 offen offset:2048 sc1\n\t" \
    "v_max3_u32 v16, v64, v65, v66\n\t" \
    "v_max3_u32 v17, v67, v68, v69\n\t" \
    "v_max3_u32 v16, v16, v70, v71\n\t" \
    "v_max_u32 v16, v16, v17\n\t" \
    "v_cmp_eq_u32_e64 s[42:43], -1, v16\n\t" \
    "s_cmp_lg_u64 s[42:43], 0\n\t" \
    "s_cbranch_scc0 92f\n\t" \
    "buffer_load_dwordx4 v[64:67], v9, s[56:59], s49 offen offset:1024 sc1\n\t" \
    "buffer_load_dwordx4 v[68:71], v9, s[56:59], s49 offen offset:3072 sc1\n\t" \
    "92:\n\t" \
    "v_max3_u32 v16, v72, v73, v74\n\t" \
    "v_max3_u32 v17, v75, v76, v77\n\t" \
    "v_max3_u32 v16, v16, v78, v79\n\t" \
    "v_max_u32 v16, v16, v17\n\t" \
    "v_cmp_eq_u32_e64 s[42:43], -1, v16\n\t" \
    "s_cmp_lg_u64 s[42:43], 0\n\t" \
    "s_cbranch_scc0 93f\n\t" \
    "buffer_load_dwordx4 v[72:75], v9, s[56:59], s51 offen offset:1024 sc1\n\t" \
    "buffer_load_dwordx4 v[76:79], v9, s[56:59], s51 offen offset:3072 sc1\n\t" \
    "93:\n\t" \
    "s_waitcnt vmcnt(0)\n\t" \
    "v_max3_u32 v16, v56, v57, v58\n\t" \
    "v_max3_u32 v17, v59, v60, v61\n\t" \
    "v_max3_u32 v16, v16, v62, v63\n\t" \
    "v_max_u32 v16, v16, v17\n\t" \
    "v_cmp_eq_u32_e64 s[42:43], -1, v16\n\t" \
    "s_cmp_lg_u64 s[42:43], 0\n\t" \
    "s_cbranch_scc1 71b\n\t" \
    "s_branch 81b\n\t" \
    "72:\n\t" \
    "s_add_i32 s50, s50, -1\n\t" \
    "s_cmp_le_i32 s50, 0\n\t" \
    "s_cbranch_scc1 82b\n\t" \
    "s_waitcnt vmcnt(0)\n\t" \
    "s_sleep 2\n\t" \
    "buffer_load_dwordx4 v[64:67], v9, s[56:59], s49 offen offset:1024 sc1\n\t" \
    "buffer_load_dwordx4 v[68:71], v9, s[56:59], s49 offen offset:3072 sc1\n\t" \
    "v_max3_u32 v16, v72, v73, v74\n\t" \
    "v_max3_u32 v17, v75, v76, v77\n\t" \
    "v_max3_u32 v16, v16, v78, v79\n\t" \
    "v_max_u32 v16, v16, v17\n\t" \
    "v_cmp_eq_u32_e64 s[42:43], -1, v16\n\t" \
    "s_cmp_lg_u64 s[42:43], 0\n\t" \
    "s_cbranch_scc0 93f\n\t" \
    "buffer_load_dwordx4 v[72:75], v9, s[56:59], s51 offen offset:1024 sc1\n\t" \
    "buffer_load_dwordx4 v[76:79], v9, s[56:59], s51 offen offset:3072 sc1\n\t" \
    "93:\n\t" \
    "s_waitcnt vmcnt(0)\n\t" \
    "v_max3_u32 v16, v64, v65, v66\n\t" \
    "v_max3_u32 v17, v67, v68, v69\n\t" \
    "v_max3_u32 v16, v16, v70, v71\n\t" \
    "v_max_u32 v16, v16, v17\n\t" \
    "v_cmp_eq_u32_e64 s[42:43], -1, v16\n\t" \
    "s_cmp_lg_u64 s[42:43], 0\n\t" \
    "s_cbranch_scc1 72b\n\t" \
    "s_branch 82b\n\t" \
    "73:\n\t" \
    "s_add_i32 s50, s50, -1\n\t" \
    "s_cmp_le_i32 s50, 0\n\t" \
    "s_cbranch_scc1 83b\n\t" \
    "s_waitcnt vmcnt(0)\n\t" \
    "s_sleep 2\n\t" \
    "buffer_load_dwordx4 v[72:75], v9, s[56:59], s51 offen offset:1024 sc1\n\t" \
    "buffer_load_dwordx4 v[76:79], v9, s[56:59], s51 offen offset:3072 sc1\n\t" \
    "s_waitcnt vmcnt(0)\n\t" \
    "v_max3_u32 v16, v72, v73, v74\n\t" \
    "v_max3_u32 v17, v75, v76, v77\n\t" \
    "v_max3_u32 v16, v16, v78, v79\n\t" \
    "v_max_u32 v16, v16, v17\n\t" \
    "v_cmp_eq_u32_e64 s[42:43], -1, v16\n\t" \
    "s_cmp_lg_u64 s[42:43], 0\n\t" \
    "s_cbranch_scc1 73b\n\t" \
    "s_branch 83b\n\t" \
    "9:\n\t" \
    "s_nop 1\n\t" \
    ""
#define PART1_CLOBBERS "v16", "v17", "v18", "v19", "v32", "v33", "v34", "v35", "v36", "v37", "v38", "v39", "v40", "v41", "v42", "v43", "v44", "v45", "v46", "v47", "v96", "v97", "v98", "v99", "v100", "v101", "v102", "v103", "v104", "v105", "v106", "v107", "v108", "v109", "v110", "v111", "v112", "v113", "v114", "v115", "v116", "v117", "v118", "v119", "v120", "v121", "v122", "v123", "v124", "v125", "v126", "v127", "v128", "v129", "v130", "v131", "v132", "v133", "v134", "v135", "v136", "v137", "v138", "v139", "v140", "v141", "v142", "v143", "v144", "v145", "v146", "v147", "v148", "v149", "v150", "v151", "v152", "v153", "v154", "v155", "v156", "v157", "v158", "v159", "v160", "v161", "v162", "v163", "v164", "v165", "v166", "v167", "v168", "v169", "v170", "v171", "v172", "v173", "v174", "v175", "v224", "v225", "v226", "v227", "v228", "v229", "v230", "v231", "v232", "v233", "v234", "v235", "v236", "v237", "v238", "v239", "v240", "v241", "v242", "v243", "v244", "v245", "v246", "v247", "v248", "v249", "v250", "v251", "v252", "v253", "v254", "v255", "s40", "s41", "s42", "s43", "s44", "s45", "s46", "s47", "s48", "s49", "s50", "s51", "vcc", "scc", "memory"
#define PART2_CLOBBERS "v20", "v21", "v22", "v23", "v224", "v225", "v226", "v227", "v228", "v229", "v230", "v231", "v232", "v233", "v234", "v235", "v236", "v237", "v238", "v239", "v240", "v241", "v242", "v243", "v244", "v245", "v246", "v247", "v248", "v249", "v250", "v251", "v252", "v253", "v254", "v255", "s52", "s53", "s40", "s41", "s42", "s43", "s44", "s45", "s46", "s47", "s48", "s49", "s50", "s51", "vcc", "scc", "memory"
#define PART3_CLOBBERS "v20", "v21", "v22", "v23", "v32", "v33", "v34", "v35", "v36", "v37", "v38", "v39", "v40", "v41", "v42", "v43", "v44", "v45", "v46", "v47", "v48", "v49", "v50", "v51", "v52", "v53", "v54", "v55", "v56", "v57", "v58", "v59", "v60", "v61", "v62", "v63", "v64", "v65", "v66", "v67", "v68", "v69", "v70", "v71", "v72", "v73", "v74", "v75", "v76", "v77", "v78", "v79", "v224", "v225", "v226", "v227", "v228", "v229", "v230", "v231", "v232", "v233", "v234", "v235", "v236", "v237", "v238", "v239", "v240", "v241", "v242", "v243", "v244", "v245", "v246", "v247", "v248", "v249", "v250", "v251", "v252", "v253", "v254", "v255", "s40", "s41", "s42", "s43", "s44", "s45", "s46", "s47", "s48", "s49", "s50", "s51", "vcc", "scc", "memory"


constexpr unsigned WS_EPOCH = 0, WS_FKV = 4096  , WS_FO = 12288  ;
constexpr unsigned WS_KV = 1u << 20  , KVB = 4u << 20, KVB_HEAD = 65 * 2048  ;
constexpr unsigned WS_O = 10u << 20  ;
constexpr unsigned L_GATEW = 131072, L_GPART = 132096, L_MTAB = 133120  , L_COMB = 134144, L_SLOT = 131072, L_TOTAL = 163840;
constexpr int JOFF = 3, JX = 1;
constexpr int CODE_BYTES = 16384;

DI int lane_id() { int l; asm volatile("v_mbcnt_lo_u32_b32 %0, -1, 0\n\tv_mbcnt_hi_u32_b32 %0, -1, %0" : "=v"(l)); return l; }
DI unsigned lo32(const void* p) { return (unsigned)(u64)p; }
DI unsigned hi32(const void* p) { return (unsigned)((u64)p >> 32); }
DI u32x4 srd4(const void* p, unsigned bytes) { return (u32x4){lo32(p), hi32(p) & 0xffffu, bytes, 0x20000u}; }
DI void dma1(unsigned lds, unsigned voff, u32x4 srd, unsigned soff) {
  asm volatile("s_mov_b32 m0, %0\n\ts_nop 0\n\tbuffer_load_dwordx4 %1, %2, %3 offen lds" :: "s"(lds), "v"(voff), "s"(srd), "s"(soff) : "memory");
}

DI f32x16 safe_tile(bf16x8 q, const char* kvhead, int kt_hi, int kt_lo, int qpos, bool windowed, int lane) {
  const __amdgpu_buffer_rsrc_t srd = __builtin_amdgcn_make_buffer_rsrc((void*)kvhead, (short)0, 131072, 0x00020000);
  const int h = lane >> 5, r = lane & 31;
  const unsigned ones = (r == 16 || r == 20) ? 0x3f803f80u : 0u;
  f32x16 o; for (int i = 0; i < 16; ++i) o[i] = 0.f;
  float m = -__builtin_inff();
#pragma unroll 1
  for (int kt = kt_hi; kt >= kt_lo; --kt) {
    const int to = (windowed ? 63 - kt : kt) * 2048;
    u32x4 kf = __builtin_amdgcn_raw_buffer_load_b128(srd, lane * 16, to, 16);
    u32x4 v0 = __builtin_amdgcn_raw_buffer_load_b128(srd, 1024 + h * 512 + (r & 15) * 16, to, 16);
    u32x4 v1 = __builtin_amdgcn_raw_buffer_load_b128(srd, 1280 + h * 512 + (r & 15) * 16, to, 16);
    if (r >= 16) { v0 = (u32x4){ones, ones, ones, ones}; v1 = v0; }
    f32x16 z; for (int i = 0; i < 16; ++i) z[i] = 0.f;
    f32x16 s = __builtin_amdgcn_mfma_f32_32x32x16_bf16(__builtin_bit_cast(bf16x8, kf), q, z, 0, 0, 0);
    float mx = m;
    for (int i = 0; i < 16; ++i) {
      const int dlt = qpos - (kt * 32 + 4 * h + (i & 3) + 8 * (i >> 2));
      if (windowed && (dlt < 0 || dlt >= 256)) s[i] = -__builtin_inff();
      mx = fmaxf(mx, s[i]);
    }
    mx = fmaxf(mx, __shfl_xor(mx, 32));
    const float alpha = __builtin_amdgcn_exp2f(m - mx);
    m = mx;
    for (int i = 0; i < 16; ++i) { o[i] *= alpha; s[i] = __builtin_amdgcn_exp2f(s[i] - mx); }
    f32x8 p0 = __builtin_shufflevector(s, s, 0, 1, 2, 3, 4, 5, 6, 7), p1 = __builtin_shufflevector(s, s, 8, 9, 10, 11, 12, 13, 14, 15);
    o = __builtin_amdgcn_smfmac_f32_32x32x32_bf16(__builtin_bit_cast(bf16x8, v0), __builtin_bit_cast(bf16x16, p0), o, 0xdddd, 0, 0);
    o = __builtin_amdgcn_smfmac_f32_32x32x32_bf16(__builtin_bit_cast(bf16x8, v1), __builtin_bit_cast(bf16x16, p1), o, 0xdddd, 0, 0);
  }
  return o;
}

__global__ void __launch_bounds__(512, 2)
gated_mix_kernel(const char* x, const char* wqkvA, const char* woA, const char* wqkvB, const char* woB, const char* gatew, char* out, char* ws) {
  u32x16 ka;
  asm volatile("s_load_dwordx16 %0, %1, 0x0" : "={s[8:23]}"(ka) : "s"(__builtin_amdgcn_kernarg_segment_ptr()));
  const int lane = threadIdx.x & 63, w = __builtin_amdgcn_readfirstlane(threadIdx.x >> 6), bid = blockIdx.x;
  const int xcd = bid & 7, tb = bid >> 3, j = xcd ^ JOFF, jb = j ^ JX;
  const int hh = w >> 1, th = w & 1, head = 4 * j + hh, headb = 4 * jb + hh, qt = 2 * tb + th;
  const int rot = (j + tb) & 3;
  unsigned vE = w * (16 * 3072) + (lane >> 3) * 3072 + (((lane & 7) ^ (((lane >> 4) & 1) << 1)) << 4), vO = (vE ^ 64) + 8 * 3072;
  unsigned vX = w * 8192 + (lane >> 4) * 1024 + (((lane & 15) ^ (lane >> 4) ^ ((w & 1) << 3)) << 4), vX2 = (vX ^ 64) + 4096;
  asm volatile("" : "+v"(vE), "+v"(vO), "+v"(vX), "+v"(vX2));
  asm volatile("s_waitcnt lgkmcnt(0)" : "+{s[8:23]}"(ka));
#define KARG(i) (char*)(((u64)ka[2 * (i) + 1] << 32) | ka[2 * (i)])
  x = KARG(0); wqkvA = KARG(1); woA = KARG(2); wqkvB = KARG(3); woB = KARG(4); gatew = KARG(5); out = KARG(6); ws = KARG(7);
  const u32x4 sA = srd4(wqkvA + j * 128, 512 * 3072 - j * 128), sX = srd4(x + tb * 65536, 65536);
  u32x4 sB = srd4(wqkvB + jb * 128, 512 * 3072 - jb * 128);
  dma1(w * 2048, vX, sX, rot * 256); dma1(w * 2048 + 1024, vX2, sX, rot * 256);
  {
    u64 pc; asm volatile("s_getpc_b64 %0" : "=s"(pc));
    u64 tp = (u64)(wqkvA + j * 128) + (unsigned)tb * 49152 + (w - 2) * 12288;
    tp = w < 2 ? (pc & ~127ull) + (w * 8 + (tb & 7)) * (CODE_BYTES / 16) : tp;
    tp = w == 7 ? (u64)gatew : tp;
    const int sh = w < 2 ? 7 : w == 7 ? 4 : 10;
    asm volatile("s_mov_b32 m0, %0\n\ts_cmp_ge_u32 %3, 6\n\ts_cbranch_scc1 1f\n\tbuffer_load_dword %1, %2, 0 offen lds\n\ts_branch 2f\n\t"
                 "1:\n\ts_cmp_lg_u32 %3, 7\n\ts_cbranch_scc1 2f\n\tbuffer_load_dwordx4 %1, %2, 0 offen lds\n\t2:"
                 :: "s"(w == 7 ? L_GATEW : L_COMB), "v"(lane << sh), "s"(srd4((const void*)tp, sh == 10 ? 12288 - 896 : 1024)), "s"(w) : "scc", "memory");
  }
  dma1(16384 + w * 2048, vE, sA, 1024 + rot * 393216); dma1(16384 + w * 2048 + 1024, vO, sA, 1024 + rot * 393216);
  const int c = lane & 15, g = lane >> 4, q = (lane >> 2) & 3, p = lane & 3, r = lane & 31, h = lane >> 5;
  const int tb3 = xcd * 4 + (tb >> 3), cb3 = tb & 7, ts = w & 3, kh = w >> 2;
  *(LAS float*)(size_t)(L_MTAB + (threadIdx.x & 255) * 4) = (threadIdx.x & 255) - 32u < 64u ? -__builtin_inff() : 0.f;
  {
    const unsigned wrd = (16 * (g >> 1) + 4 * (g & 1) + q) * 128 + ((((g & 1) << 1) | (q >> 1)) << 5) + p * 8 + kh * 32768 + 65536;
    const unsigned pk = L_COMB + 1024 + (w ^ 1) * 1024 + lane * 16;
    unsigned po_a = tb3 * 128, po_b = (tb * 16 + j) * 8; asm("" : "+s"(po_a), "+s"(po_b));
    *(LAS u32x4*)(size_t)(pk + 8192) = (u32x4){wrd, wrd ^ 32, wrd ^ 64, wrd ^ 96};
    *(LAS u32x4*)(size_t)(pk + 16384) = (u32x4){L_SLOT + w * 4096 + lane * 16, lane < 8 ? po_a + lane * 8 : po_b,
                                                (unsigned)(r >> 4) * 16384 + h * 256 + c * 16, L_GPART + th * 128 + r * 4};
  }
  ws = (char*)(((u64)ws + 0x1ffffffull) & ~0x1ffffffull);
  const unsigned alo = lo32(ws), ahi = hi32(ws);
  const int wx = w ^ ((tb & 8) >> 1), wst = (tb & 16) ? -(wx * 65536 + 7 * 8192) : wx * 65536;
  const int lo = tb >= 4 ? tb - 4 : 0;
  u32x16 vi;
  vi[0] = (8 * g + q) * 128 + ((hh ^ ((q >> 1) | ((g & 1) << 1))) << 5) + p * 8;
  vi[1] = vi[0] + 65536;
  vi[2] = vE; vi[3] = vO - 1024;
  vi[4] = (th * 32 + c) * 256 + ((g ^ c) << 4);
  vi[5] = vX2 - 1024; vi[11] = vX;
  vi[6] = L_GPART + hh * 256 + th * 128 + c * 4;
  vi[7] = L_COMB + 1024 + w * 1024 + lane * 16;
  vi[8] = (r == 16 || r == 20) ? 0x3f803f80u : 0u;
  vi[9] = lane * 16;
  vi[10] = th * 65536 + h * 512 + c * 16;
  vi[12] = L_GATEW + ((hh + rot) & 3) * 256 + g * 16;
  vi[13] = L_MTAB + (31 - r + 4 * h) * 4;
  vi[14] = c * 8; vi[15] = (lane <= tb - lo ? lane : 0) * 8;
  const char* wt = (bid & 1 ? woB : woA) + (bid >> 1) * 4096 + w * 512;
  const unsigned fl = alo + WS_FKV + j * 256, tB = KVB + headb * KVB_HEAD + (63 - qt) * 2048, sa = alo + WS_KV + head * 131072 + (tb & 15) * 4096;
  const u32x8 s56 = {alo + WS_KV, ahi & 0xffffu, 9u << 20, 0x20000u, (unsigned)head * 131072 + qt * 2048, tB, lo32(wt), hi32(wt)};
  const u32x8 s64 = {fl + tb * 8, ahi, fl + th * 128, ahi, fl + 2048 + lo * 8, ahi, sa, ahi};
  const u32x4 s72 = {alo + WS_KV + tB - th * 65536, (unsigned)(qt < 8 ? qt + 1 : 9), (unsigned)w, (unsigned)wst};
  const u32x4 s88 = {rot * 393216u, ((rot + 1) & 3) * 393216u, ((rot + 2) & 3) * 393216u, ((rot + 3) & 3) * 393216u};
  u32x2 tg, bad; u32x4 qa, qo, qb; f32x16 o0, o1, ob; u32x16 kring; u32x32 vring;
  asm volatile(PART1_ASM : "+{v[0:15]}"(vi), "={v[20:23]}"(qb), "={v[24:27]}"(qo), "={v[28:31]}"(qa), "={v[48:63]}"(o0), "={v[64:79]}"(o1), "={v[80:95]}"(ob),
                           "={v[176:191]}"(kring), "={v[192:223]}"(vring), "={s[36:37]}"(tg), "={s[38:39]}"(bad), "+{s[52:55]}"(sB)
               : "{s[56:63]}"(s56), "{s[64:71]}"(s64), "{s[72:75]}"(s72), "{s[76:79]}"(srd4(woB + cb3 * 128, 511 * 1024 + 128)), "{s[80:83]}"(sA), "{s[84:87]}"(sX),
                 "{s[88:91]}"(s88) : PART1_CLOBBERS);
  const u64 tag = ((u64)(unsigned)__builtin_amdgcn_readfirstlane(tg[1]) << 32) | (unsigned)__builtin_amdgcn_readfirstlane(tg[0]);
  if (__builtin_expect(__builtin_amdgcn_readfirstlane(bad[0] | bad[1]) != 0, 0)) {
    asm volatile("s_waitcnt vmcnt(0)" ::: "memory");
    const int l = lane_id();
    const f32x16 t = safe_tile(__builtin_bit_cast(bf16x8, qb), ws + WS_KV + KVB + headb * KVB_HEAD, qt, qt >= 8 ? qt - 8 : 0, qt * 32 + (l & 31), true, l);
    asm volatile("s_nop 11\n\tv_mov_b32 v80, %1\n\tv_mov_b32 v81, %2\n\tv_mov_b32 v82, %3\n\tv_mov_b32 v83, %4\n\tv_mov_b32 v84, %5\n\tv_mov_b32 v85, %6\n\t"
                 "v_mov_b32 v86, %7\n\tv_mov_b32 v87, %8\n\tv_mov_b32 v88, %9"
                 : "=&{v[80:95]}"(ob) : "v"(t[0]), "v"(t[1]), "v"(t[2]), "v"(t[3]), "v"(t[4]), "v"(t[5]), "v"(t[6]), "v"(t[7]), "v"(t[8]));
  }
  u32x4 of; u32x16 wfa; u32x32 wf1, wf2;
  const u32x4 tO = {alo + WS_O, ahi & 0xffffu, 4u << 20, 0x20000u}, tF = {alo + WS_FO + (tb * 16 + j) * 8, ahi, alo + WS_FO, ahi};
  const u32x2 tS = {(unsigned)qt * 32768 + head * 512, (unsigned)(tb3 * 4 + ts) * 16384 + kh * 8192}, tA = {sa, ahi};
  const u32x4 tOut = srd4(out, 2u << 20);
  const unsigned tOo = (tb3 * 64 + ts * 16 + kh * 8) * 1024 + cb3 * 128, tCr = L_SLOT + (w ^ 1) * 4096, tCx = L_SLOT + (ts ^ 1) * 4096 + kh * 2048;
  asm volatile(PART2_ASM : "+{v[0:15]}"(vi), "={v[16:19]}"(of), "+{v[48:63]}"(o0), "+{v[64:79]}"(o1), "+{v[176:191]}"(kring), "+{v[192:223]}"(vring),
                           "+{v[80:95]}"(ob), "={v[96:111]}"(wfa), "={v[112:143]}"(wf1), "={v[144:175]}"(wf2), "={s[38:39]}"(bad)
               : "{v[24:27]}"(qo), "{v[28:31]}"(qa), "{s[36:37]}"(tag), "{s[56:59]}"(tO), "{s[60:61]}"(tS), "{s[64:67]}"(tF), "{s[70:71]}"(tA), "{s74}"(w),
                 "{s75}"(wst), "{s[76:79]}"(srd4(woA + cb3 * 128, 511 * 1024 + 128)), "{s[84:87]}"(tOut), "{s88}"(tOo), "{s89}"(tCr), "{s90}"(tCx) : PART2_CLOBBERS);
  if (__builtin_expect(__builtin_amdgcn_readfirstlane(bad[0] | bad[1]) != 0, 0)) {
    o0 = safe_tile(__builtin_bit_cast(bf16x8, qa), ws + WS_KV + head * 131072, 63, 0, 0, false, lane_id());
    asm volatile(PART2R_ASM : "={v[176:191]}"(kring), "={v[192:223]}"(vring), "={v[80:95]}"(ob), "={v[96:111]}"(wfa), "={v[112:143]}"(wf1), "={v[144:175]}"(wf2),
                              "+{v[0:15]}"(vi), "={v[16:19]}"(of), "+{v[48:63]}"(o0)
                 : "{s[56:59]}"(tO), "{s[60:61]}"(tS) : "s49", "s51", "scc", "memory");
  }
  asm volatile(PART3_ASM : "+{v[0:15]}"(vi), "+{v[16:19]}"(of), "+{v[80:95]}"(ob), "+{v[96:111]}"(wfa), "+{v[112:143]}"(wf1), "+{v[144:175]}"(wf2)
               : "{v[176:191]}"(kring), "{v[192:223]}"(vring), "{s[56:59]}"(tO), "{s[60:61]}"(tS), "{s[84:87]}"(tOut), "{s88}"(tOo), "{s89}"(tCr), "{s90}"(tCx)
               : PART3_CLOBBERS);
  if (bid == 0 && w == 0 && lane_id() == 0) __hip_atomic_store((GAS u64*)(ws + WS_EPOCH), tag, __ATOMIC_RELAXED, __HIP_MEMORY_SCOPE_AGENT);
}

extern "C" void kernel_launch(void* const* d_in, const int* in_sizes, int n_in, void* d_out, int out_size, void* d_ws, size_t ws_size, hipStream_t stream) {
  (void)in_sizes; (void)n_in; (void)out_size; (void)ws_size;
  (void)hipFuncSetAttribute((const void*)gated_mix_kernel, hipFuncAttributeMaxDynamicSharedMemorySize, L_TOTAL);
  gated_mix_kernel<<<256, 512, L_TOTAL, stream>>>((const char*)d_in[0], (const char*)d_in[1], (const char*)d_in[2], (const char*)d_in[4],
                                                  (const char*)d_in[5], (const char*)d_in[6], (char*)d_out, (char*)d_ws);
}
```
